```python
import torch

def _softmax(x, axis=-1):
    return torch.softmax(x, dim=axis)
def _gelu(x):
    return torch.nn.functional.gelu(x, approximate="tanh")
def _silu(x):
    return torch.nn.functional.silu(x)
def _sigmoid(x):
    return torch.sigmoid(x)
def _layernorm(x, axis=-1, eps=1e-5):
    m = x.mean(dim=axis, keepdim=True)
    v = x.var(dim=axis, keepdim=True, unbiased=False)
    return (x - m) / torch.sqrt(v + eps)
def _scatter_add(tgt, idx, src, axis=0):
    return tgt.clone().index_add_(axis, idx, src)
def _segment_sum(src, seg, n):
    out = torch.zeros((n,) + src.shape[1:], dtype=src.dtype)
    return out.index_add_(0, seg, src)
def _pad(x, pw):
    flat = tuple(v for pair in reversed(pw) for v in pair)
    return torch.nn.functional.pad(x, flat)

kernel_name = 'standalone__ssm_conv_s5__prompt'

def setup_inputs():

    _w = (lambda a: (lambda t: t.to(torch.bfloat16) if t.is_floating_point() else t)(torch.as_tensor(a)))
    B, S, D = 1, 2048, 1024
    P = D
    Lambda_re = -0.5 * (lambda _x: torch.exp(torch.as_tensor(_x)))((lambda *s: torch.randn(*s, dtype=torch.bfloat16) * 0.2)(P))
    Lambda_im = (lambda *s: torch.randn(*s, dtype=torch.bfloat16) * 0.4)(P) * 1.0
    B_re = (lambda *s: torch.randn(*s, dtype=torch.bfloat16) * 0.2)(P, D) * (D ** -0.5)
    B_im = (lambda *s: torch.randn(*s, dtype=torch.bfloat16) * 0.2)(P, D) * (D ** -0.5)
    C_re = (lambda *s: torch.randn(*s, dtype=torch.bfloat16) * 0.2)(D, P) * (P ** -0.5)
    C_im = (lambda *s: torch.randn(*s, dtype=torch.bfloat16) * 0.2)(D, P) * (P ** -0.5)
    log_dt = (lambda *s: torch.randn(*s, dtype=torch.bfloat16) * 0.2)(P) * 0.3 - 2.0
    Dskip = (lambda *s: torch.randn(*s, dtype=torch.bfloat16) * 0.05)(D)
    return {
        "x": _w((lambda *s: torch.randn(*s, dtype=torch.bfloat16) * 0.05)(B, S, D)),
        "Lambda_re": _w(Lambda_re),
        "Lambda_im": _w(Lambda_im),
        "B_re": _w(B_re),
        "B_im": _w(B_im),
        "C_re": _w(C_re),
        "C_im": _w(C_im),
        "log_dt": _w(log_dt),
        "Dskip": _w(Dskip),
    }

def reference(x, Lambda_re, Lambda_im, B_re, B_im, C_re, C_im, log_dt, Dskip):
    Bsz, L, H = x.shape
    Lam = Lambda_re + 1j * Lambda_im
    Bmat = B_re + 1j * B_im
    Cmat = C_re + 1j * C_im
    dt = torch.exp(log_dt)
    Lam_bar = torch.exp(dt * Lam)
    B_bar = ((Lam_bar - 1.0) / Lam)[:, None] * Bmat
    Bu = torch.einsum("bld,pd->blp", x + 0j, B_bar)
    h = torch.zeros_like(Bu[:, 0, :])
    hs = []
    for t in range(L):
        h = Lam_bar[None, :] * h + Bu[:, t, :]
        hs.append(h)
    hseq = torch.stack(hs, 1)
    y = 2.0 * torch.einsum("blp,dp->bld", hseq, Cmat).real + x * Dskip[None, None, :]
    return y
```

```cpp
#include <hip/hip_runtime.h>
#include <hip/hip_bf16.h>
#include <stdint.h>

typedef __attribute__((ext_vector_type(8))) short bf16x8;
typedef __attribute__((ext_vector_type(4))) float f32x4;
typedef unsigned short u16;

static constexpr int L = 2048, D = 1024, P = 1024;

__device__ __forceinline__ float bf2f(u16 v) { return __uint_as_float(((unsigned)v) << 16); }
__device__ __forceinline__ u16 f2bf(float f) {
  __hip_bfloat16 b = __float2bfloat16(f);
  return *reinterpret_cast<u16*>(&b);
}

__global__ void prep_kernel(const u16* Lre, const u16* Lim, const u16* logdt, float4* consts) {
  int p = blockIdx.x * blockDim.x + threadIdx.x;
  if (p >= P) return;
  float lr = bf2f(Lre[p]), li = bf2f(Lim[p]);
  float dt = bf2f(f2bf(expf(bf2f(logdt[p]))));
  float ar = dt * lr, ai = dt * li;
  float e = expf(ar);
  float br = e * cosf(ai), bi = e * sinf(ai);
  float nr = br - 1.0f, ni = bi;
  float den = lr * lr + li * li;
  float cr = (nr * lr + ni * li) / den;
  float ci = (ni * lr - nr * li) / den;
  consts[p] = make_float4(br, bi, cr, ci);
}

template <int MODE>
__global__ __launch_bounds__(256) void gemm_simple(const u16* A, int lda, const u16* B0, const u16* B1, int K,
                                                   float* Cout, int ldc, const u16* x, const u16* Dskip) {
  int wid = threadIdx.x >> 6, lane = threadIdx.x & 63;
  int wr = wid >> 1, wc = wid & 1;
  int m0 = blockIdx.y * 64 + wr * 32, n0 = blockIdx.x * 64 + wc * 32;
  f32x4 acc[2][2] = {};
  int fr = lane & 15, fq = lane >> 4;
  for (int k0 = 0; k0 < K; k0 += 32) {
    bf16x8 a[2], b[2];
    for (int i = 0; i < 2; ++i) a[i] = *(const bf16x8*)(A + (size_t)(m0 + i * 16 + fr) * lda + k0 + fq * 8);
    for (int j = 0; j < 2; ++j) {
      int n = n0 + j * 16 + fr;
      const u16* bp;
      if (MODE == 0) {
        bp = (n < P ? B0 + (size_t)n * D : B1 + (size_t)(n - P) * D) + k0 + fq * 8;
      } else {
        bp = (k0 < P ? B0 + (size_t)n * P + k0 : B1 + (size_t)n * P + (k0 - P)) + fq * 8;
      }
      b[j] = *(const bf16x8*)bp;
    }
    for (int i = 0; i < 2; ++i)
      for (int j = 0; j < 2; ++j) acc[i][j] = __builtin_amdgcn_mfma_f32_16x16x32_bf16(a[i], b[j], acc[i][j], 0, 0, 0);
  }
  for (int i = 0; i < 2; ++i)
    for (int j = 0; j < 2; ++j)
      for (int r = 0; r < 4; ++r) {
        int m = m0 + i * 16 + fq * 4 + r, n = n0 + j * 16 + fr;
        float v = acc[i][j][r];
        if (MODE == 1) {
          float sk = bf2f(f2bf(bf2f(x[(size_t)m * D + n]) * bf2f(Dskip[n])));
          v = 2.0f * v + sk;
        }
        Cout[(size_t)m * ldc + n] = v;
      }
}

__global__ void scan_simple(const float* Bu, const float4* consts, u16* h) {
  int p = blockIdx.x * blockDim.x + threadIdx.x;
  if (p >= P) return;
  float4 c = consts[p];
  float hr = 0.f, hi = 0.f;
  for (int t = 0; t < L; ++t) {
    float ur0 = Bu[(size_t)t * 2048 + p], ui0 = Bu[(size_t)t * 2048 + P + p];
    float ur = c.z * ur0 - c.w * ui0, ui = c.z * ui0 + c.w * ur0;
    float nr = c.x * hr - c.y * hi + ur;
    float ni = c.x * hi + c.y * hr + ui;
    hr = nr; hi = ni;
    h[(size_t)t * 2048 + p] = f2bf(hr);
    h[(size_t)t * 2048 + P + p] = f2bf(-hi);
  }
}

extern "C" void kernel_launch(void* const* d_in, const int* in_sizes, int n_in, void* d_out, int out_size,
                              void* d_ws, size_t ws_size, hipStream_t stream) {
  const u16* x = (const u16*)d_in[0];
  const u16* Lre = (const u16*)d_in[1];
  const u16* Lim = (const u16*)d_in[2];
  const u16* Bre = (const u16*)d_in[3];
  const u16* Bim = (const u16*)d_in[4];
  const u16* Cre = (const u16*)d_in[5];
  const u16* Cim = (const u16*)d_in[6];
  const u16* logdt = (const u16*)d_in[7];
  const u16* Dskip = (const u16*)d_in[8];
  float* out = (float*)d_out;
  char* ws = (char*)d_ws;
  float4* consts = (float4*)ws;
  float* Bu = (float*)(ws + (1 << 20));
  u16* h = (u16*)(ws + (32 << 20));
  prep_kernel<<<4, 256, 0, stream>>>(Lre, Lim, logdt, consts);
  gemm_simple<0><<<dim3(2048 / 64, L / 64), 256, 0, stream>>>(x, D, Bre, Bim, D, Bu, 2048, nullptr, nullptr);
  scan_simple<<<16, 64, 0, stream>>>(Bu, consts, h);
  gemm_simple<1><<<dim3(D / 64, L / 64), 256, 0, stream>>>(h, 2048, Cre, Cim, 2048, out, D, x, Dskip);
}
```

```cpp
#include <hip/hip_runtime.h>
#include <hip/hip_bf16.h>
#include <stdint.h>

typedef __attribute__((ext_vector_type(8))) short bf16x8;
typedef __attribute__((ext_vector_type(4))) short s16x4;
typedef __attribute__((ext_vector_type(4))) float f32x4;
typedef __attribute__((ext_vector_type(4))) unsigned u32x4;
typedef __attribute__((ext_vector_type(2))) unsigned u32x2;
typedef unsigned short u16;
typedef unsigned long long u64;
#define LDSP(T, off) ((__attribute__((address_space(3))) T*)(smem + (off)))

static constexpr int L = 2048, D = 1024, P = 1024;

__device__ __forceinline__ float bf2f(u16 v) { return __uint_as_float(((unsigned)v) << 16); }
__device__ __forceinline__ float bfround(float f) { return __bfloat162float(__float2bfloat16(f)); }
__device__ __forceinline__ unsigned pack2(float a, float b) {
  __hip_bfloat16 x = __float2bfloat16(a), y = __float2bfloat16(b);
  return (unsigned)(*reinterpret_cast<u16*>(&x)) | ((unsigned)(*reinterpret_cast<u16*>(&y)) << 16);
}
#define WAIT_V(n) asm volatile("s_waitcnt vmcnt(" #n ")" ::: "memory")
#define WAIT_L0() asm volatile("s_waitcnt lgkmcnt(0)" ::: "memory")
#define BAR() asm volatile("s_barrier" ::: "memory")
#define MFMA_DRAIN() do { __builtin_amdgcn_sched_barrier(0); asm volatile("s_nop 15\n\ts_nop 15"); __builtin_amdgcn_sched_barrier(0); } while (0)

struct cd { double r, i; };
__device__ __forceinline__ cd cmul(cd a, cd b) { return cd{a.r * b.r - a.i * b.i, a.r * b.i + a.i * b.r}; }

static constexpr int K1_NS = 4, K1_STAGE = 32768, K1_S = D / 64;
static constexpr int K1_LDS = K1_NS * K1_STAGE;
static constexpr int IMG_PITCH = 288;

__global__ __launch_bounds__(512, 2) void k1_gemm_scan(const u16* __restrict__ x, const u16* __restrict__ Bre,
                                                       const u16* __restrict__ Bim, const u16* __restrict__ Lre,
                                                       const u16* __restrict__ Lim, const u16* __restrict__ logdt,
                                                       u16* __restrict__ h, u64* exch) {
  extern __shared__ __attribute__((aligned(16))) char smem[];
  const int b = blockIdx.x, xcd = b & 7, idx = b >> 3;
  const int c = 4 * (xcd & 3) + (idx & 3), g = 8 * (xcd >> 2) + (idx >> 2);
  const int wave = __builtin_amdgcn_readfirstlane(threadIdx.x >> 6), lane = threadIdx.x & 63;
  if (wave >= 4) {
    const int lw = wave - 4;
    const u16* src[8];
#pragma unroll
    for (int i = 0; i < 4; ++i) {
      int rho = 32 * lw + 8 * i + (lane >> 3);
      int chunk = (lane & 7) ^ ((rho >> 1) & 7);
      int m = rho >> 4, i16 = rho & 15;
      int tau = 32 * (i16 >> 2) + 4 * m + (i16 & 3);
      src[i] = x + (size_t)(128 * c + tau) * D + chunk * 8;
      int p = 64 * g + 16 * (rho >> 5) + (rho & 15);
      src[4 + i] = ((rho & 16) ? Bim : Bre) + (size_t)p * D + chunk * 8;
    }
    auto issue = [&](int s) {
      int buf = (s & (K1_NS - 1)) * K1_STAGE;
#pragma unroll
      for (int i = 0; i < 4; ++i)
        __builtin_amdgcn_global_load_lds((const unsigned*)(src[i] + s * 64), LDSP(unsigned, buf + (32 * lw + 8 * i) * 128), 16, 0, 0);
#pragma unroll
      for (int i = 0; i < 4; ++i)
        __builtin_amdgcn_global_load_lds((const unsigned*)(src[4 + i] + s * 64), LDSP(unsigned, buf + 16384 + (32 * lw + 8 * i) * 128), 16, 0, 0);
    };
    issue(0); issue(1); issue(2);
    for (int s = 0; s < K1_S - 2; ++s) {
      WAIT_V(16); BAR();
      if (s + 3 < K1_S) issue(s + 3);
    }
    WAIT_V(8); BAR();
    WAIT_V(0); BAR();
    return;
  }
  const int w = wave, fr = lane & 15, fq = lane >> 4;
  const int p = 64 * g + 16 * w + fr;
  float lamr, lami, cfr, cfi;
  float l32r, l32i, l64r, l64i, l96r, l96i;
  float mur[4], mui[4];
  {
    float lr = bf2f(Lre[p]), li = bf2f(Lim[p]);
    float dt = bfround(expf(bf2f(logdt[p])));
    float ar = dt * lr, ai = dt * li;
    float e = expf(ar);
    lamr = e * cosf(ai); lami = e * sinf(ai);
    float nr = lamr - 1.0f, ni = lami;
    float den = lr * lr + li * li;
    cfr = (nr * lr + ni * li) / den;
    cfi = (ni * lr - nr * li) / den;
    cd a{(double)lamr, (double)lami};
#pragma unroll
    for (int i = 0; i < 5; ++i) a = cmul(a, a);
    cd a32 = a, a64 = cmul(a, a), a96 = cmul(a64, a32), mu = cmul(a64, a64);
    l32r = (float)a32.r; l32i = (float)a32.i; l64r = (float)a64.r; l64i = (float)a64.i; l96r = (float)a96.r; l96i = (float)a96.i;
    cd mu2 = cmul(mu, mu), mu4 = cmul(mu2, mu2), mu8 = cmul(mu4, mu4);
#pragma unroll
    for (int i = 0; i < 4; ++i) {
      int e2 = c - 1 - fq - 4 * i;
      cd r{1.0, 0.0};
      if (e2 & 1) r = cmul(r, mu);
      if (e2 & 2) r = cmul(r, mu2);
      if (e2 & 4) r = cmul(r, mu4);
      if (e2 & 8) r = cmul(r, mu8);
      mur[i] = e2 >= 0 ? (float)r.r : 0.f; mui[i] = e2 >= 0 ? (float)r.i : 0.f;
    }
  }
  asm volatile("" : "+v"(lamr), "+v"(lami), "+v"(cfr), "+v"(cfi), "+v"(l32r), "+v"(l32i), "+v"(l64r), "+v"(l64i), "+v"(l96r), "+v"(l96i));
  asm volatile("" : "+v"(mur[0]), "+v"(mur[1]), "+v"(mur[2]), "+v"(mur[3]), "+v"(mui[0]), "+v"(mui[1]), "+v"(mui[2]), "+v"(mui[3]));
  f32x4 acc[8][2];
#pragma unroll
  for (int m = 0; m < 8; ++m) { acc[m][0] = f32x4{0, 0, 0, 0}; acc[m][1] = f32x4{0, 0, 0, 0}; }
  const int c0 = fq ^ (fr >> 1);
  const int a0 = fr * 128 + c0 * 16, a1 = fr * 128 + (c0 ^ 4) * 16;
  bf16x8 F0[10], F1[10];
  auto rd = [&](bf16x8* F, int base) {
#pragma unroll
    for (int n = 0; n < 2; ++n) F[8 + n] = *LDSP(bf16x8, base + 16384 + 4096 * w + 2048 * n);
#pragma unroll
    for (int m = 0; m < 8; ++m) F[m] = *LDSP(bf16x8, base + 2048 * m);
  };
  auto mm = [&](bf16x8* F) {
#pragma unroll
    for (int m = 0; m < 8; ++m)
#pragma unroll
      for (int n = 0; n < 2; ++n) acc[m][n] = __builtin_amdgcn_mfma_f32_16x16x32_bf16(F[m], F[8 + n], acc[m][n], 0, 0, 0);
  };
#define ILV(NR, NM)                                                        \
  _Pragma("unroll") for (int i_ = 0; i_ < NR; ++i_) {                       \
    __builtin_amdgcn_sched_group_barrier(0x8, 1, 0);                        \
    __builtin_amdgcn_sched_group_barrier(0x100, 1, 0);                      \
  }                                                                         \
  __builtin_amdgcn_sched_group_barrier(0x8, NM - NR, 0);                    \
  __builtin_amdgcn_sched_barrier(0);
  BAR();
  rd(F0, a0);
  __builtin_amdgcn_sched_barrier(0);
  rd(F1, a1);
  mm(F0);
  ILV(10, 16)
  WAIT_L0();
  for (int s = 1; s < K1_S; ++s) {
    int buf = (s & (K1_NS - 1)) * K1_STAGE;
    BAR();
    __builtin_amdgcn_sched_barrier(0);
    rd(F0, buf + a0);
    mm(F1);
    ILV(10, 16)
    rd(F1, buf + a1);
    mm(F0);
    ILV(10, 16)
    WAIT_L0();
  }
  mm(F1);
  MFMA_DRAIN();

#pragma unroll
  for (int m = 0; m < 8; ++m)
#pragma unroll
    for (int j = 0; j < 4; ++j) {
      float a = acc[m][0][j], bb = acc[m][1][j];
      acc[m][0][j] = cfr * a - cfi * bb;
      acc[m][1][j] = cfr * bb + cfi * a;
    }
  float sr = 0.f, si = 0.f;
#pragma unroll
  for (int m = 0; m < 8; ++m)
#pragma unroll
    for (int j = 0; j < 4; ++j) {
      float nr = lamr * sr - lami * si + acc[m][0][j];
      float ni = lamr * si + lami * sr + acc[m][1][j];
      sr = nr; si = ni;
    }
  float ar[4], ai[4];
#pragma unroll
  for (int q = 0; q < 4; ++q) { ar[q] = __shfl(sr, fr + 16 * q); ai[q] = __shfl(si, fr + 16 * q); }
  float e1r = ar[0], e1i = ai[0];
  float e2r = l32r * e1r - l32i * e1i + ar[1], e2i = l32r * e1i + l32i * e1r + ai[1];
  float e3r = l32r * e2r - l32i * e2i + ar[2], e3i = l32r * e2i + l32i * e2r + ai[2];
  float agr = l32r * e3r - l32i * e3i + ar[3], agi = l32r * e3i + l32i * e3r + ai[3];
  if (fq == 0 && c < 15) {
    u64 bits = (u64)__float_as_uint(agr) | ((u64)__float_as_uint(agi) << 32);
    if (bits == ~0ull) bits = 0x7fc000007fc00000ull;
    __hip_atomic_store(&exch[c * P + p], bits, __ATOMIC_RELAXED, __HIP_MEMORY_SCOPE_AGENT);
  }
  float er = fq == 0 ? 0.f : fq == 1 ? e1r : fq == 2 ? e2r : e3r;
  float ei = fq == 0 ? 0.f : fq == 1 ? e1i : fq == 2 ? e2i : e3i;
  float cr = 0.f, ci = 0.f;
  if (c > 0) {
    u64 v[4];
    for (unsigned spin = 0;; ++spin) {
      bool ok = true;
#pragma unroll
      for (int i = 0; i < 4; ++i) {
        int j = min(fq + 4 * i, c - 1);
        v[i] = __hip_atomic_load(&exch[j * P + p], __ATOMIC_RELAXED, __HIP_MEMORY_SCOPE_AGENT);
        ok &= (v[i] != ~0ull);
      }
      if (__all(ok) || spin > (1u << 24)) break;
      __builtin_amdgcn_s_sleep(1);
    }
#pragma unroll
    for (int i = 0; i < 4; ++i) {
      float vr = __uint_as_float((unsigned)v[i]), vi = __uint_as_float((unsigned)(v[i] >> 32));
      if (fq + 4 * i < c) { cr += mur[i] * vr - mui[i] * vi; ci += mur[i] * vi + mui[i] * vr; }
    }
    cr += __shfl_xor(cr, 16); ci += __shfl_xor(ci, 16);
    cr += __shfl_xor(cr, 32); ci += __shfl_xor(ci, 32);
  }
  {
    float pr = fq == 0 ? 1.f : fq == 1 ? l32r : fq == 2 ? l64r : l96r;
    float pi = fq == 0 ? 0.f : fq == 1 ? l32i : fq == 2 ? l64i : l96i;
    sr = pr * cr - pi * ci + er;
    si = pr * ci + pi * cr + ei;
  }
#pragma unroll
  for (int m = 0; m < 8; ++m)
#pragma unroll
    for (int j = 0; j < 4; ++j) {
      float nr = lamr * sr - lami * si + acc[m][0][j];
      float ni = lamr * si + lami * sr + acc[m][1][j];
      sr = nr; si = ni;
      acc[m][0][j] = nr; acc[m][1][j] = -ni;
    }
#pragma unroll
  for (int n = 0; n < 2; ++n)
#pragma unroll
    for (int i = 0; i < 4; ++i) {
      u32x4 v;
      v[0] = pack2(acc[2 * i][n][0], acc[2 * i][n][1]);
      v[1] = pack2(acc[2 * i][n][2], acc[2 * i][n][3]);
      v[2] = pack2(acc[2 * i + 1][n][0], acc[2 * i + 1][n][1]);
      v[3] = pack2(acc[2 * i + 1][n][2], acc[2 * i + 1][n][3]);
      *LDSP(u32x4, (64 * n + 16 * w + fr) * IMG_PITCH + 64 * fq + 16 * i) = v;
    }
  WAIT_L0();
  BAR();
  {
    const int a = (lane >> 2) & 3, bb = lane & 3;
#pragma unroll
    for (int tb = 0; tb < 2; ++tb)
#pragma unroll
      for (int cq = 0; cq < 4; ++cq) {
        int chunk = 4 * cq + fq;
        int tcol = (32 * w + 16 * tb + 4 * bb) * 2;
        int r1 = 8 * chunk + ((fq & 1) ? 4 : 0) + a, r2 = 8 * chunk + ((fq & 1) ? 0 : 4) + a;
        s16x4 v1 = __builtin_amdgcn_ds_read_tr16_b64_v4i16(LDSP(s16x4, r1 * IMG_PITCH + tcol));
        s16x4 v2 = __builtin_amdgcn_ds_read_tr16_b64_v4i16(LDSP(s16x4, r2 * IMG_PITCH + tcol));
        s16x4 lo = (fq & 1) ? v2 : v1, hi = (fq & 1) ? v1 : v2;
        bf16x8 o = __builtin_shufflevector(lo, hi, 0, 1, 2, 3, 4, 5, 6, 7);
        int t = 128 * c + 32 * w + 16 * tb + fr;
        *(bf16x8*)(h + (size_t)t * 2048 + 128 * g + 8 * chunk) = o;
      }
  }
}

static constexpr int K2_NS = 6, K2_STAGE = 24576, K2_S = 32;
static constexpr int K2_LDS = K2_NS * K2_STAGE;

__global__ __launch_bounds__(512, 2) void k2_gemm(const u16* __restrict__ h, const u16* __restrict__ Cre,
                                                  const u16* __restrict__ Cim, const u16* __restrict__ x,
                                                  const u16* __restrict__ Dskip, float* __restrict__ y) {
  extern __shared__ __attribute__((aligned(16))) char smem[];
  const int b = blockIdx.x, xcd = b & 7, idx = b >> 3;
  const int c = 4 * (xcd & 3) + (idx & 3), n = 8 * (xcd >> 2) + (idx >> 2);
  const int wave = __builtin_amdgcn_readfirstlane(threadIdx.x >> 6), lane = threadIdx.x & 63;
  if (wave >= 4) {
    const int lw = wave - 4;
    const u16* srcH[4];
    int offC[2];
#pragma unroll
    for (int i = 0; i < 4; ++i) {
      int rho = 32 * lw + 8 * i + (lane >> 3);
      int chunk = (lane & 7) ^ ((rho >> 1) & 7);
      srcH[i] = h + (size_t)(128 * c + rho) * 2048 + chunk * 8;
    }
#pragma unroll
    for (int i = 0; i < 2; ++i) {
      int rho = 16 * lw + 8 * i + (lane >> 3);
      int chunk = (lane & 7) ^ ((rho >> 1) & 7);
      offC[i] = (64 * n + rho) * P + chunk * 8;
    }
    auto issue = [&](int s, int bi) {
      int buf = bi * K2_STAGE;
      const u16* Cb = ((s & 1) ? Cim : Cre) + (s >> 1) * 64;
#pragma unroll
      for (int i = 0; i < 4; ++i)
        __builtin_amdgcn_global_load_lds((const unsigned*)(srcH[i] + s * 64), LDSP(unsigned, buf + (32 * lw + 8 * i) * 128), 16, 0, 0);
#pragma unroll
      for (int i = 0; i < 2; ++i)
        __builtin_amdgcn_global_load_lds((const unsigned*)(Cb + offC[i]), LDSP(unsigned, buf + 16384 + (16 * lw + 8 * i) * 128), 16, 0, 0);
    };
#pragma unroll
    for (int s = 0; s < K2_NS - 1; ++s) issue(s, s);
    int bi = K2_NS - 1;
    for (int s = 0; s < K2_S - 4; ++s) {
      WAIT_V(24); BAR();
      if (s + K2_NS - 1 < K2_S) { issue(s + K2_NS - 1, bi); bi = bi == K2_NS - 1 ? 0 : bi + 1; }
    }
    WAIT_V(18); BAR();
    WAIT_V(12); BAR();
    WAIT_V(6); BAR();
    WAIT_V(0); BAR();
    return;
  }
  const int wd = wave & 1, wt = wave >> 1, fr = lane & 15, fq = lane >> 4;
  u32x2 xs[2][4], ds[2];
#pragma unroll
  for (int dm = 0; dm < 2; ++dm) {
    int d = 64 * n + 32 * wd + 16 * dm + 4 * fq;
    ds[dm] = *(const u32x2*)(Dskip + d);
#pragma unroll
    for (int tn = 0; tn < 4; ++tn) xs[dm][tn] = *(const u32x2*)(x + (size_t)(128 * c + 64 * wt + 16 * tn + fr) * D + d);
  }
  f32x4 acc[2][4];
#pragma unroll
  for (int i = 0; i < 2; ++i)
#pragma unroll
    for (int j = 0; j < 4; ++j) acc[i][j] = f32x4{0, 0, 0, 0};
  const int c0 = fq ^ (fr >> 1);
  const int a0 = fr * 128 + c0 * 16, a1 = fr * 128 + (c0 ^ 4) * 16;
  bf16x8 F0[6], F1[6];
  auto rd = [&](bf16x8* F, int base) {
#pragma unroll
    for (int i = 0; i < 2; ++i) F[i] = *LDSP(bf16x8, base + 16384 + (32 * wd + 16 * i) * 128);
#pragma unroll
    for (int j = 0; j < 4; ++j) F[2 + j] = *LDSP(bf16x8, base + (64 * wt + 16 * j) * 128);
  };
  auto mm = [&](bf16x8* F) {
#pragma unroll
    for (int i = 0; i < 2; ++i)
#pragma unroll
      for (int j = 0; j < 4; ++j) acc[i][j] = __builtin_amdgcn_mfma_f32_16x16x32_bf16(F[i], F[2 + j], acc[i][j], 0, 0, 0);
  };
  BAR();
  rd(F0, a0);
  __builtin_amdgcn_sched_barrier(0);
  rd(F1, a1);
  mm(F0);
  ILV(6, 8)
  WAIT_L0();
  int buf = K2_STAGE;
  for (int s = 1; s < K2_S; ++s) {
    BAR();
    __builtin_amdgcn_sched_barrier(0);
    rd(F0, buf + a0);
    mm(F1);
    ILV(6, 8)
    rd(F1, buf + a1);
    mm(F0);
    ILV(6, 8)
    WAIT_L0();
    buf = buf == (K2_NS - 1) * K2_STAGE ? 0 : buf + K2_STAGE;
  }
  mm(F1);
  MFMA_DRAIN();
#pragma unroll
  for (int dm = 0; dm < 2; ++dm) {
    int d = 64 * n + 32 * wd + 16 * dm + 4 * fq;
    float dk[4] = {__uint_as_float(ds[dm][0] << 16), __uint_as_float(ds[dm][0] & 0xffff0000u),
                   __uint_as_float(ds[dm][1] << 16), __uint_as_float(ds[dm][1] & 0xffff0000u)};
#pragma unroll
    for (int tn = 0; tn < 4; ++tn) {
      float xv[4] = {__uint_as_float(xs[dm][tn][0] << 16), __uint_as_float(xs[dm][tn][0] & 0xffff0000u),
                     __uint_as_float(xs[dm][tn][1] << 16), __uint_as_float(xs[dm][tn][1] & 0xffff0000u)};
      f32x4 o;
#pragma unroll
      for (int j = 0; j < 4; ++j) o[j] = 2.0f * acc[dm][tn][j] + bfround(xv[j] * dk[j]);
      *(f32x4*)(y + (size_t)(128 * c + 64 * wt + 16 * tn + fr) * D + d) = o;
    }
  }
}

__global__ __launch_bounds__(256) void k0_init(u32x4* p) { p[blockIdx.x * 256 + threadIdx.x] = u32x4{~0u, ~0u, ~0u, ~0u}; }

extern "C" void kernel_launch(void* const* d_in, const int* in_sizes, int n_in, void* d_out, int out_size,
                              void* d_ws, size_t ws_size, hipStream_t stream) {
  const u16* x = (const u16*)d_in[0];
  const u16* Lre = (const u16*)d_in[1];
  const u16* Lim = (const u16*)d_in[2];
  const u16* Bre = (const u16*)d_in[3];
  const u16* Bim = (const u16*)d_in[4];
  const u16* Cre = (const u16*)d_in[5];
  const u16* Cim = (const u16*)d_in[6];
  const u16* logdt = (const u16*)d_in[7];
  const u16* Dskip = (const u16*)d_in[8];
  float* out = (float*)d_out;
  char* ws = (char*)d_ws;
  u64* exch = (u64*)ws;
  u16* h = (u16*)(ws + (1 << 20));
  hipFuncSetAttribute((const void*)k1_gemm_scan, hipFuncAttributeMaxDynamicSharedMemorySize, K1_LDS);
  hipFuncSetAttribute((const void*)k2_gemm, hipFuncAttributeMaxDynamicSharedMemorySize, K2_LDS);
  k0_init<<<32, 256, 0, stream>>>((u32x4*)exch);
  k1_gemm_scan<<<256, 512, K1_LDS, stream>>>(x, Bre, Bim, Lre, Lim, logdt, h, exch);
  k2_gemm<<<256, 512, K2_LDS, stream>>>(h, Cre, Cim, x, Dskip, out);
}
```

```cpp
#include <hip/hip_runtime.h>
#include <hip/hip_bf16.h>
#include <stdint.h>

typedef __attribute__((ext_vector_type(8))) short bf16x8;
typedef __attribute__((ext_vector_type(4))) short s16x4;
typedef __attribute__((ext_vector_type(4))) float f32x4;
typedef __attribute__((ext_vector_type(4))) unsigned u32x4;
typedef __attribute__((ext_vector_type(2))) unsigned u32x2;
typedef unsigned short u16;
typedef unsigned long long u64;
#define LDSP(T, off) ((__attribute__((address_space(3))) T*)(smem + (off)))

static constexpr int L = 2048, D = 1024, P = 1024;

__device__ __forceinline__ float bf2f(u16 v) { return __uint_as_float(((unsigned)v) << 16); }
__device__ __forceinline__ float bfround(float f) { return __bfloat162float(__float2bfloat16(f)); }
__device__ __forceinline__ unsigned pack2(float a, float b) {
  __hip_bfloat16 x = __float2bfloat16(a), y = __float2bfloat16(b);
  return (unsigned)(*reinterpret_cast<u16*>(&x)) | ((unsigned)(*reinterpret_cast<u16*>(&y)) << 16);
}
#define WAIT_V(n) asm volatile("s_waitcnt vmcnt(" #n ")" ::: "memory")
#define WAIT_L0() asm volatile("s_waitcnt lgkmcnt(0)" ::: "memory")
#define BAR() asm volatile("s_barrier" ::: "memory")
#define MFMA_DRAIN() do { __builtin_amdgcn_sched_barrier(0); asm volatile("s_nop 15\n\ts_nop 15"); __builtin_amdgcn_sched_barrier(0); } while (0)

struct cd { double r, i; };
__device__ __forceinline__ cd cmul(cd a, cd b) { return cd{a.r * b.r - a.i * b.i, a.r * b.i + a.i * b.r}; }

static constexpr int K1_NS = 4, K1_STAGE = 32768, K1_S = D / 64;
static constexpr int K1_LDS = K1_NS * K1_STAGE;
static constexpr int IMG_PITCH = 288;

__global__ __launch_bounds__(512, 2) void k1_gemm_scan(const u16* __restrict__ x, const u16* __restrict__ Bre,
                                                       const u16* __restrict__ Bim, const u16* __restrict__ Lre,
                                                       const u16* __restrict__ Lim, const u16* __restrict__ logdt,
                                                       u16* __restrict__ h, u64* exch) {
  extern __shared__ __attribute__((aligned(16))) char smem[];
  const int b = blockIdx.x, xcd = b & 7, idx = b >> 3;
  const int c = 4 * (xcd & 3) + (idx & 3), g = 8 * (xcd >> 2) + (idx >> 2);
  const int wave = __builtin_amdgcn_readfirstlane(threadIdx.x >> 6), lane = threadIdx.x & 63;
  if (wave >= 4) {
    const int lw = wave - 4;
    const u16* src[8];
#pragma unroll
    for (int i = 0; i < 4; ++i) {
      int rho = 32 * lw + 8 * i + (lane >> 3);
      int chunk = (lane & 7) ^ ((rho >> 1) & 7);
      int m = rho >> 4, i16 = rho & 15;
      int tau = 32 * (i16 >> 2) + 4 * m + (i16 & 3);
      src[i] = x + (size_t)(128 * c + tau) * D + chunk * 8;
      int p = 64 * g + 16 * (rho >> 5) + (rho & 15);
      src[4 + i] = ((rho & 16) ? Bim : Bre) + (size_t)p * D + chunk * 8;
    }
    const int koff = 2 * (idx >> 2) + 4 * (idx & 3);
    auto issue = [&](int s0) {
      int buf = (s0 & (K1_NS - 1)) * K1_STAGE;
      int s = (s0 + koff) & 15;
#pragma unroll
      for (int i = 0; i < 4; ++i)
        __builtin_amdgcn_global_load_lds((const unsigned*)(src[i] + s * 64), LDSP(unsigned, buf + (32 * lw + 8 * i) * 128), 16, 0, 0);
#pragma unroll
      for (int i = 0; i < 4; ++i)
        __builtin_amdgcn_global_load_lds((const unsigned*)(src[4 + i] + s * 64), LDSP(unsigned, buf + 16384 + (32 * lw + 8 * i) * 128), 16, 0, 0);
    };
    issue(0); issue(1); issue(2);
    for (int s = 0; s < K1_S - 2; ++s) {
      WAIT_V(16); BAR();
      if (s + 3 < K1_S) issue(s + 3);
    }
    WAIT_V(8); BAR();
    WAIT_V(0); BAR();
    return;
  }
  const int w = wave, fr = lane & 15, fq = lane >> 4;
  const int p = 64 * g + 16 * w + fr;
  float lamr, lami, cfr, cfi;
  float l32r, l32i, l64r, l64i, l96r, l96i;
  float mur[4], mui[4];
  {
    float lr = bf2f(Lre[p]), li = bf2f(Lim[p]);
    float dt = bfround(expf(bf2f(logdt[p])));
    float ar = dt * lr, ai = dt * li;
    float e = expf(ar);
    lamr = e * cosf(ai); lami = e * sinf(ai);
    float nr = lamr - 1.0f, ni = lami;
    float den = lr * lr + li * li;
    cfr = (nr * lr + ni * li) / den;
    cfi = (ni * lr - nr * li) / den;
    cd a{(double)lamr, (double)lami};
#pragma unroll
    for (int i = 0; i < 5; ++i) a = cmul(a, a);
    cd a32 = a, a64 = cmul(a, a), a96 = cmul(a64, a32), mu = cmul(a64, a64);
    l32r = (float)a32.r; l32i = (float)a32.i; l64r = (float)a64.r; l64i = (float)a64.i; l96r = (float)a96.r; l96i = (float)a96.i;
    cd mu2 = cmul(mu, mu), mu4 = cmul(mu2, mu2), mu8 = cmul(mu4, mu4);
#pragma unroll
    for (int i = 0; i < 4; ++i) {
      int e2 = c - 1 - fq - 4 * i;
      cd r{1.0, 0.0};
      if (e2 & 1) r = cmul(r, mu);
      if (e2 & 2) r = cmul(r, mu2);
      if (e2 & 4) r = cmul(r, mu4);
      if (e2 & 8) r = cmul(r, mu8);
      mur[i] = e2 >= 0 ? (float)r.r : 0.f; mui[i] = e2 >= 0 ? (float)r.i : 0.f;
    }
  }
  asm volatile("" : "+v"(lamr), "+v"(lami), "+v"(cfr), "+v"(cfi), "+v"(l32r), "+v"(l32i), "+v"(l64r), "+v"(l64i), "+v"(l96r), "+v"(l96i));
  asm volatile("" : "+v"(mur[0]), "+v"(mur[1]), "+v"(mur[2]), "+v"(mur[3]), "+v"(mui[0]), "+v"(mui[1]), "+v"(mui[2]), "+v"(mui[3]));
  f32x4 acc[8][2];
#pragma unroll
  for (int m = 0; m < 8; ++m) { acc[m][0] = f32x4{0, 0, 0, 0}; acc[m][1] = f32x4{0, 0, 0, 0}; }
  const int c0 = fq ^ (fr >> 1);
  const int a0 = fr * 128 + c0 * 16, a1 = fr * 128 + (c0 ^ 4) * 16;
  bf16x8 F0[10], F1[10];
  auto rd = [&](bf16x8* F, int base) {
#pragma unroll
    for (int n = 0; n < 2; ++n) F[8 + n] = *LDSP(bf16x8, base + 16384 + 4096 * w + 2048 * n);
#pragma unroll
    for (int m = 0; m < 8; ++m) F[m] = *LDSP(bf16x8, base + 2048 * m);
  };
  auto mm = [&](bf16x8* F) {
#pragma unroll
    for (int m = 0; m < 8; ++m)
#pragma unroll
      for (int n = 0; n < 2; ++n) acc[m][n] = __builtin_amdgcn_mfma_f32_16x16x32_bf16(F[m], F[8 + n], acc[m][n], 0, 0, 0);
  };
#define ILV(NR, NM)                                                        \
  _Pragma("unroll") for (int i_ = 0; i_ < NR; ++i_) {                       \
    __builtin_amdgcn_sched_group_barrier(0x8, 1, 0);                        \
    __builtin_amdgcn_sched_group_barrier(0x100, 1, 0);                      \
  }                                                                         \
  __builtin_amdgcn_sched_group_barrier(0x8, NM - NR, 0);                    \
  __builtin_amdgcn_sched_barrier(0);
  BAR();
  rd(F0, a0);
  __builtin_amdgcn_sched_barrier(0);
  rd(F1, a1);
  mm(F0);
  ILV(10, 16)
  WAIT_L0();
  for (int s = 1; s < K1_S; ++s) {
    int buf = (s & (K1_NS - 1)) * K1_STAGE;
    BAR();
    __builtin_amdgcn_sched_barrier(0);
    rd(F0, buf + a0);
    mm(F1);
    ILV(10, 16)
    rd(F1, buf + a1);
    mm(F0);
    ILV(10, 16)
    WAIT_L0();
  }
  mm(F1);
  MFMA_DRAIN();

#pragma unroll
  for (int m = 0; m < 8; ++m)
#pragma unroll
    for (int j = 0; j < 4; ++j) {
      float a = acc[m][0][j], bb = acc[m][1][j];
      acc[m][0][j] = cfr * a - cfi * bb;
      acc[m][1][j] = cfr * bb + cfi * a;
    }
  float sr = 0.f, si = 0.f;
#pragma unroll
  for (int m = 0; m < 8; ++m)
#pragma unroll
    for (int j = 0; j < 4; ++j) {
      float nr = lamr * sr - lami * si + acc[m][0][j];
      float ni = lamr * si + lami * sr + acc[m][1][j];
      sr = nr; si = ni;
    }
  float ar[4], ai[4];
#pragma unroll
  for (int q = 0; q < 4; ++q) { ar[q] = __shfl(sr, fr + 16 * q); ai[q] = __shfl(si, fr + 16 * q); }
  float e1r = ar[0], e1i = ai[0];
  float e2r = l32r * e1r - l32i * e1i + ar[1], e2i = l32r * e1i + l32i * e1r + ai[1];
  float e3r = l32r * e2r - l32i * e2i + ar[2], e3i = l32r * e2i + l32i * e2r + ai[2];
  float agr = l32r * e3r - l32i * e3i + ar[3], agi = l32r * e3i + l32i * e3r + ai[3];
  if (fq == 0 && c < 15) {
    u64 bits = (u64)__float_as_uint(agr) | ((u64)__float_as_uint(agi) << 32);
    if (bits == ~0ull) bits = 0x7fc000007fc00000ull;
    __hip_atomic_store(&exch[c * P + p], bits, __ATOMIC_RELAXED, __HIP_MEMORY_SCOPE_AGENT);
  }
  float er = fq == 0 ? 0.f : fq == 1 ? e1r : fq == 2 ? e2r : e3r;
  float ei = fq == 0 ? 0.f : fq == 1 ? e1i : fq == 2 ? e2i : e3i;
  float cr = 0.f, ci = 0.f;
  if (c > 0) {
    u64 v[4];
    for (unsigned spin = 0;; ++spin) {
      bool ok = true;
#pragma unroll
      for (int i = 0; i < 4; ++i) {
        int j = min(fq + 4 * i, c - 1);
        v[i] = __hip_atomic_load(&exch[j * P + p], __ATOMIC_RELAXED, __HIP_MEMORY_SCOPE_AGENT);
        ok &= (v[i] != ~0ull);
      }
      if (__all(ok) || spin > (1u << 24)) break;
      __builtin_amdgcn_s_sleep(1);
    }
#pragma unroll
    for (int i = 0; i < 4; ++i) {
      float vr = __uint_as_float((unsigned)v[i]), vi = __uint_as_float((unsigned)(v[i] >> 32));
      if (fq + 4 * i < c) { cr += mur[i] * vr - mui[i] * vi; ci += mur[i] * vi + mui[i] * vr; }
    }
    cr += __shfl_xor(cr, 16); ci += __shfl_xor(ci, 16);
    cr += __shfl_xor(cr, 32); ci += __shfl_xor(ci, 32);
  }
  {
    float pr = fq == 0 ? 1.f : fq == 1 ? l32r : fq == 2 ? l64r : l96r;
    float pi = fq == 0 ? 0.f : fq == 1 ? l32i : fq == 2 ? l64i : l96i;
    sr = pr * cr - pi * ci + er;
    si = pr * ci + pi * cr + ei;
  }
#pragma unroll
  for (int m = 0; m < 8; ++m)
#pragma unroll
    for (int j = 0; j < 4; ++j) {
      float nr = lamr * sr - lami * si + acc[m][0][j];
      float ni = lamr * si + lami * sr + acc[m][1][j];
      sr = nr; si = ni;
      acc[m][0][j] = nr; acc[m][1][j] = -ni;
    }
#pragma unroll
  for (int n = 0; n < 2; ++n)
#pragma unroll
    for (int i = 0; i < 4; ++i) {
      u32x4 v;
      v[0] = pack2(acc[2 * i][n][0], acc[2 * i][n][1]);
      v[1] = pack2(acc[2 * i][n][2], acc[2 * i][n][3]);
      v[2] = pack2(acc[2 * i + 1][n][0], acc[2 * i + 1][n][1]);
      v[3] = pack2(acc[2 * i + 1][n][2], acc[2 * i + 1][n][3]);
      *LDSP(u32x4, (64 * n + 16 * w + fr) * IMG_PITCH + 64 * fq + 16 * i) = v;
    }
  WAIT_L0();
  BAR();
  {
    const int a = (lane >> 2) & 3, bb = lane & 3;
#pragma unroll
    for (int tb = 0; tb < 2; ++tb)
#pragma unroll
      for (int cq = 0; cq < 4; ++cq) {
        int chunk = 4 * cq + fq;
        int tcol = (32 * w + 16 * tb + 4 * bb) * 2;
        int r1 = 8 * chunk + ((fq & 1) ? 4 : 0) + a, r2 = 8 * chunk + ((fq & 1) ? 0 : 4) + a;
        s16x4 v1 = __builtin_amdgcn_ds_read_tr16_b64_v4i16(LDSP(s16x4, r1 * IMG_PITCH + tcol));
        s16x4 v2 = __builtin_amdgcn_ds_read_tr16_b64_v4i16(LDSP(s16x4, r2 * IMG_PITCH + tcol));
        s16x4 lo = (fq & 1) ? v2 : v1, hi = (fq & 1) ? v1 : v2;
        bf16x8 o = __builtin_shufflevector(lo, hi, 0, 1, 2, 3, 4, 5, 6, 7);
        int t = 128 * c + 32 * w + 16 * tb + fr;
        *(bf16x8*)(h + (size_t)t * 2048 + 128 * g + 8 * chunk) = o;
      }
  }
}

static constexpr int K2_NS = 6, K2_STAGE = 24576, K2_S = 32;
static constexpr int K2_LDS = K2_NS * K2_STAGE;

__global__ __launch_bounds__(512, 2) void k2_gemm(const u16* __restrict__ h, const u16* __restrict__ Cre,
                                                  const u16* __restrict__ Cim, const u16* __restrict__ x,
                                                  const u16* __restrict__ Dskip, float* __restrict__ y) {
  extern __shared__ __attribute__((aligned(16))) char smem[];
  const int b = blockIdx.x, xcd = b & 7, idx = b >> 3;
  const int c = 4 * (xcd & 3) + (idx & 3), n = 8 * (xcd >> 2) + (idx >> 2);
  const int wave = __builtin_amdgcn_readfirstlane(threadIdx.x >> 6), lane = threadIdx.x & 63;
  if (wave >= 4) {
    const int lw = wave - 4;
    const u16* srcH[4];
    int offC[2];
#pragma unroll
    for (int i = 0; i < 4; ++i) {
      int rho = 32 * lw + 8 * i + (lane >> 3);
      int chunk = (lane & 7) ^ ((rho >> 1) & 7);
      srcH[i] = h + (size_t)(128 * c + rho) * 2048 + chunk * 8;
    }
#pragma unroll
    for (int i = 0; i < 2; ++i) {
      int rho = 16 * lw + 8 * i + (lane >> 3);
      int chunk = (lane & 7) ^ ((rho >> 1) & 7);
      offC[i] = (64 * n + rho) * P + chunk * 8;
    }
    const int koff = 4 * (idx >> 2) + 8 * (idx & 3);
    auto issue = [&](int s0, int bi) {
      int buf = bi * K2_STAGE;
      int s = (s0 + koff) & 31;
      const u16* Cb = ((s & 1) ? Cim : Cre) + (s >> 1) * 64;
#pragma unroll
      for (int i = 0; i < 4; ++i)
        __builtin_amdgcn_global_load_lds((const unsigned*)(srcH[i] + s * 64), LDSP(unsigned, buf + (32 * lw + 8 * i) * 128), 16, 0, 0);
#pragma unroll
      for (int i = 0; i < 2; ++i)
        __builtin_amdgcn_global_load_lds((const unsigned*)(Cb + offC[i]), LDSP(unsigned, buf + 16384 + (16 * lw + 8 * i) * 128), 16, 0, 0);
    };
#pragma unroll
    for (int s = 0; s < K2_NS - 1; ++s) issue(s, s);
    int bi = K2_NS - 1;
    for (int s = 0; s < K2_S - 4; ++s) {
      WAIT_V(24); BAR();
      if (s + K2_NS - 1 < K2_S) { issue(s + K2_NS - 1, bi); bi = bi == K2_NS - 1 ? 0 : bi + 1; }
    }
    WAIT_V(18); BAR();
    WAIT_V(12); BAR();
    WAIT_V(6); BAR();
    WAIT_V(0); BAR();
    return;
  }
  const int wd = wave & 1, wt = wave >> 1, fr = lane & 15, fq = lane >> 4;
  u32x2 xs[2][4], ds[2];
#pragma unroll
  for (int dm = 0; dm < 2; ++dm) {
    int d = 64 * n + 32 * wd + 16 * dm + 4 * fq;
    ds[dm] = *(const u32x2*)(Dskip + d);
#pragma unroll
    for (int tn = 0; tn < 4; ++tn) xs[dm][tn] = *(const u32x2*)(x + (size_t)(128 * c + 64 * wt + 16 * tn + fr) * D + d);
  }
  f32x4 acc[2][4];
#pragma unroll
  for (int i = 0; i < 2; ++i)
#pragma unroll
    for (int j = 0; j < 4; ++j) acc[i][j] = f32x4{0, 0, 0, 0};
  const int c0 = fq ^ (fr >> 1);
  const int a0 = fr * 128 + c0 * 16, a1 = fr * 128 + (c0 ^ 4) * 16;
  bf16x8 F0[6], F1[6];
  auto rd = [&](bf16x8* F, int base) {
#pragma unroll
    for (int i = 0; i < 2; ++i) F[i] = *LDSP(bf16x8, base + 16384 + (32 * wd + 16 * i) * 128);
#pragma unroll
    for (int j = 0; j < 4; ++j) F[2 + j] = *LDSP(bf16x8, base + (64 * wt + 16 * j) * 128);
  };
  auto mm = [&](bf16x8* F) {
#pragma unroll
    for (int i = 0; i < 2; ++i)
#pragma unroll
      for (int j = 0; j < 4; ++j) acc[i][j] = __builtin_amdgcn_mfma_f32_16x16x32_bf16(F[i], F[2 + j], acc[i][j], 0, 0, 0);
  };
  BAR();
  rd(F0, a0);
  __builtin_amdgcn_sched_barrier(0);
  rd(F1, a1);
  mm(F0);
  ILV(6, 8)
  WAIT_L0();
  int buf = K2_STAGE;
  for (int s = 1; s < K2_S; ++s) {
    BAR();
    __builtin_amdgcn_sched_barrier(0);
    rd(F0, buf + a0);
    mm(F1);
    ILV(6, 8)
    rd(F1, buf + a1);
    mm(F0);
    ILV(6, 8)
    WAIT_L0();
    buf = buf == (K2_NS - 1) * K2_STAGE ? 0 : buf + K2_STAGE;
  }
  mm(F1);
  MFMA_DRAIN();
#pragma unroll
  for (int dm = 0; dm < 2; ++dm) {
    int d = 64 * n + 32 * wd + 16 * dm + 4 * fq;
    float dk[4] = {__uint_as_float(ds[dm][0] << 16), __uint_as_float(ds[dm][0] & 0xffff0000u),
                   __uint_as_float(ds[dm][1] << 16), __uint_as_float(ds[dm][1] & 0xffff0000u)};
#pragma unroll
    for (int tn = 0; tn < 4; ++tn) {
      float xv[4] = {__uint_as_float(xs[dm][tn][0] << 16), __uint_as_float(xs[dm][tn][0] & 0xffff0000u),
                     __uint_as_float(xs[dm][tn][1] << 16), __uint_as_float(xs[dm][tn][1] & 0xffff0000u)};
      f32x4 o;
#pragma unroll
      for (int j = 0; j < 4; ++j) o[j] = 2.0f * acc[dm][tn][j] + bfround(xv[j] * dk[j]);
      *(f32x4*)(y + (size_t)(128 * c + 64 * wt + 16 * tn + fr) * D + d) = o;
    }
  }
}

__global__ __launch_bounds__(256) void k0_init(u32x4* p) { p[blockIdx.x * 256 + threadIdx.x] = u32x4{~0u, ~0u, ~0u, ~0u}; }

extern "C" void kernel_launch(void* const* d_in, const int* in_sizes, int n_in, void* d_out, int out_size,
                              void* d_ws, size_t ws_size, hipStream_t stream) {
  const u16* x = (const u16*)d_in[0];
  const u16* Lre = (const u16*)d_in[1];
  const u16* Lim = (const u16*)d_in[2];
  const u16* Bre = (const u16*)d_in[3];
  const u16* Bim = (const u16*)d_in[4];
  const u16* Cre = (const u16*)d_in[5];
  const u16* Cim = (const u16*)d_in[6];
  const u16* logdt = (const u16*)d_in[7];
  const u16* Dskip = (const u16*)d_in[8];
  float* out = (float*)d_out;
  char* ws = (char*)d_ws;
  u64* exch = (u64*)ws;
  u16* h = (u16*)(ws + (1 << 20));
  hipFuncSetAttribute((const void*)k1_gemm_scan, hipFuncAttributeMaxDynamicSharedMemorySize, K1_LDS);
  hipFuncSetAttribute((const void*)k2_gemm, hipFuncAttributeMaxDynamicSharedMemorySize, K2_LDS);
  k0_init<<<32, 256, 0, stream>>>((u32x4*)exch);
  k1_gemm_scan<<<256, 512, K1_LDS, stream>>>(x, Bre, Bim, Lre, Lim, logdt, h, exch);
  k2_gemm<<<256, 512, K2_LDS, stream>>>(h, Cre, Cim, x, Dskip, out);
}
```

```cpp
#include <hip/hip_runtime.h>
#include <hip/hip_bf16.h>
#include <stdint.h>

typedef __attribute__((ext_vector_type(8))) short bf16x8;
typedef __attribute__((ext_vector_type(4))) short s16x4;
typedef __attribute__((ext_vector_type(4))) float f32x4;
typedef __attribute__((ext_vector_type(2))) float f32x2;
typedef __attribute__((ext_vector_type(4))) unsigned u32x4;
typedef __attribute__((ext_vector_type(2))) unsigned u32x2;
typedef unsigned short u16;
typedef unsigned long long u64;
#define LDSP(T, off) ((__attribute__((address_space(3))) T*)(smem + (off)))

#define STAMP(i)
#define LSTAMP(i)
#define TSARG
#define TSPASS

static constexpr int D = 1024, P = 1024;

__device__ __forceinline__ float bf2f(u16 v) { return __uint_as_float(((unsigned)v) << 16); }
__device__ __forceinline__ float bfround(float f) { return __bfloat162float(__float2bfloat16(f)); }
__device__ __forceinline__ unsigned pack2(float a, float b) {
  __hip_bfloat16 x = __float2bfloat16(a), y = __float2bfloat16(b);
  return (unsigned)(*reinterpret_cast<u16*>(&x)) | ((unsigned)(*reinterpret_cast<u16*>(&y)) << 16);
}
#define WAIT_V(n) asm volatile("s_waitcnt vmcnt(%0)" ::"i"(n) : "memory")
#define WAIT_L0() asm volatile("s_waitcnt lgkmcnt(0)" ::: "memory")
#define BAR() asm volatile("s_barrier" ::: "memory")
#define SB0() __builtin_amdgcn_sched_barrier(0)
#define MFMA_DRAIN() do { SB0(); asm volatile("s_nop 15\n\ts_nop 15"); SB0(); } while (0)
#define ILV(NR, NM)                                                        \
  _Pragma("unroll") for (int i_ = 0; i_ < NR; ++i_) {                       \
    __builtin_amdgcn_sched_group_barrier(0x8, 1, 0);                        \
    __builtin_amdgcn_sched_group_barrier(0x100, 1, 0);                      \
  }                                                                         \
  __builtin_amdgcn_sched_group_barrier(0x8, NM - NR, 0);                    \
  SB0();
#define GLDS(gp, lo) __builtin_amdgcn_global_load_lds((const unsigned*)(gp), LDSP(unsigned, (lo)), 16, 0, 0)
#define GLDS_SC1(gp, lo) __builtin_amdgcn_global_load_lds((const unsigned*)(gp), LDSP(unsigned, (lo)), 16, 0, 16)

struct cd { double r, i; };
__device__ __forceinline__ cd cmul(cd a, cd b) { return cd{a.r * b.r - a.i * b.i, a.r * b.i + a.i * b.r}; }
__device__ __forceinline__ f32x2 tof(cd a) { return f32x2{(float)a.r, (float)a.i}; }
__device__ __forceinline__ f32x2 cfma(f32x2 a, f32x2 b, f32x2 c) {
  f32x2 t = __builtin_elementwise_fma(f32x2{a.x, a.x}, b, c);
  return __builtin_elementwise_fma(f32x2{-a.y, a.y}, f32x2{b.y, b.x}, t);
}

static constexpr int S1 = 16, ST1 = 32768;
static constexpr int S2 = 32, NS2 = 6;
static constexpr int LDS_BYTES = 163840;
static constexpr int IMG_PITCH = 288;
__device__ __forceinline__ constexpr int cslot(int i) { return i < 4 ? 131072 + i * 8192 : 114688 + (i - 4) * 8192; }

__global__ __launch_bounds__(512, 2) void s5_fused(const u16* __restrict__ x, const u16* __restrict__ Bre,
                                                   const u16* __restrict__ Bim, const u16* __restrict__ Cre,
                                                   const u16* __restrict__ Cim, const u16* __restrict__ Lre,
                                                   const u16* __restrict__ Lim, const u16* __restrict__ logdt,
                                                   const u16* __restrict__ Dskip, u16* h, u64* exch,
                                                   unsigned* flags, float* __restrict__ y TSARG) {
  extern __shared__ __attribute__((aligned(16))) char smem[];
  const int b = blockIdx.x, xcd = b & 7, idx = b >> 3;
  const int c = 4 * (xcd & 3) + (idx & 3), g = 8 * (xcd >> 2) + (idx >> 2);
  const int n = g;
  const int wave = __builtin_amdgcn_readfirstlane(threadIdx.x >> 6), lane = threadIdx.x & 63;
  if (wave >= 4) {
    const int lw = wave - 4;
    const int koff2 = 0;
    int offC[2];
#pragma unroll
    for (int i = 0; i < 2; ++i) {
      int rho = 16 * lw + 8 * i + (lane >> 3);
      int chunk = (lane & 7) ^ ((rho >> 1) & 7);
      offC[i] = (64 * n + rho) * P + chunk * 8;
    }
    auto issueC = [&](int s0, int slot_off) {
      int s = (s0 + koff2) & 31;
      const u16* Cb = ((s & 1) ? Cim : Cre) + (s >> 1) * 64;
#pragma unroll
      for (int i = 0; i < 2; ++i) GLDS(Cb + offC[i], slot_off + (2 * lw + i) * 1024);
    };
    {
      const u16* src[8];
#pragma unroll
      for (int i = 0; i < 4; ++i) {
        int rho = 32 * lw + 8 * i + (lane >> 3);
        int chunk = (lane & 7) ^ ((rho >> 1) & 7);
        int m = rho >> 4, i16 = rho & 15;
        int tau = 32 * (i16 >> 2) + 4 * m + (i16 & 3);
        src[i] = x + (size_t)(128 * c + tau) * D + chunk * 8;
        int p = 64 * g + 16 * (rho >> 5) + (rho & 15);
        src[4 + i] = ((rho & 16) ? Bim : Bre) + (size_t)p * D + chunk * 8;
      }
      const int koff = 2 * (idx >> 2) + 4 * (idx & 3);
      auto issue = [&](int s0) {
        int buf = (s0 & 3) * ST1;
        int s = (s0 + koff) & 15;
#pragma unroll
        for (int i = 0; i < 4; ++i) GLDS(src[i] + s * 64, buf + (4 * lw + i) * 1024);
#pragma unroll
        for (int i = 0; i < 4; ++i) GLDS(src[4 + i] + s * 64, buf + 16384 + (4 * lw + i) * 1024);
      };
      issue(0); issue(1); issue(2);
      for (int s = 0; s < 6; ++s) { WAIT_V(16); BAR(); issue(s + 3); }
      WAIT_V(16); BAR(); issue(9);
      {
        const char* p1 = (const char*)Cre + b * 8192 + lw * 2048 + lane * 16;
        const char* p2 = (const char*)Cim + b * 8192 + lw * 2048 + lane * 16;
        GLDS(p1, 131072 + lw * 1024); GLDS(p1 + 1024, 131072 + lw * 1024);
        GLDS(p2, 131072 + lw * 1024); GLDS(p2 + 1024, 131072 + lw * 1024);
      }
      WAIT_V(20); BAR(); issue(10);
      WAIT_V(20); BAR(); issue(11);
      WAIT_V(20); BAR(); issue(12);
      WAIT_V(16); BAR(); issue(13);
      WAIT_V(16); BAR(); issue(14);
      WAIT_V(16); BAR(); issue(15);
      issueC(0, cslot(0)); issueC(1, cslot(1)); issueC(2, cslot(2)); issueC(3, cslot(3));
      WAIT_V(24); BAR();
      WAIT_V(16); BAR();
      WAIT_V(8); BAR();
    }
    const u16* srcH[4];
#pragma unroll
    for (int i = 0; i < 4; ++i) {
      int rho = 32 * lw + 8 * i + (lane >> 3);
      int chunk = (lane & 7) ^ ((rho >> 1) & 7);
      srcH[i] = h + (size_t)(128 * c + rho) * 2048 + chunk * 8;
    }
    auto issueH = [&](int s0, int bi) {
      int s = (s0 + koff2) & 31;
#pragma unroll
      for (int i = 0; i < 4; ++i) GLDS_SC1(srcH[i] + s * 64, bi * 16384 + (4 * lw + i) * 1024);
    };
    BAR();
    BAR();
    if (lw == 0) {
      for (unsigned spin = 0;; ++spin) {
        unsigned v = __hip_atomic_load(&flags[c * 64 + lane], __ATOMIC_RELAXED, __HIP_MEMORY_SCOPE_AGENT);
        if (__all(v == 1u) || spin > (1u << 22)) break;
        __builtin_amdgcn_s_sleep(4);
      }
    }
    LSTAMP(12);
    BAR();
    issueH(0, 0); issueH(1, 1); issueH(2, 2); issueH(3, 3);
    issueH(4, 4); issueC(4, cslot(4));
    WAIT_V(18); BAR(); issueH(5, 5); issueC(5, cslot(5));
    WAIT_V(20); BAR(); issueH(6, 0); issueC(6, cslot(0));
    WAIT_V(22); BAR(); issueH(7, 1); issueC(7, cslot(1));
    WAIT_V(24); BAR(); issueH(8, 2); issueC(8, cslot(2));
    for (int s = 4; s < 22; s += 6) {
      WAIT_V(24); BAR(); issueH(s + 5, 3); issueC(s + 5, cslot(3));
      WAIT_V(24); BAR(); issueH(s + 6, 4); issueC(s + 6, cslot(4));
      WAIT_V(24); BAR(); issueH(s + 7, 5); issueC(s + 7, cslot(5));
      WAIT_V(24); BAR(); issueH(s + 8, 0); issueC(s + 8, cslot(0));
      WAIT_V(24); BAR(); issueH(s + 9, 1); issueC(s + 9, cslot(1));
      WAIT_V(24); BAR(); issueH(s + 10, 2); issueC(s + 10, cslot(2));
    }
    WAIT_V(24); BAR(); issueH(27, 3); issueC(27, cslot(3));
    WAIT_V(24); BAR(); issueH(28, 4); issueC(28, cslot(4));
    WAIT_V(24); BAR(); issueH(29, 5); issueC(29, cslot(5));
    WAIT_V(24); BAR(); issueH(30, 0); issueC(30, cslot(0));
    WAIT_V(24); BAR(); issueH(31, 1); issueC(31, cslot(1));
    WAIT_V(24); BAR();
    WAIT_V(18); BAR();
    WAIT_V(12); BAR();
    WAIT_V(6); BAR();
    WAIT_V(0); BAR();
    return;
  }
  STAMP(0);
  const int w = wave, fr = lane & 15, fq = lane >> 4;
  const int c0 = fq ^ (fr >> 1);
  const int a0 = fr * 128 + c0 * 16, a1 = fr * 128 + (c0 ^ 4) * 16;
  {
    const int p = 64 * g + 16 * w + fr;
    f32x2 lam, cf, l8, l16, l24, l32, lq, mu_[4];
    {
      float lr = bf2f(Lre[p]), li = bf2f(Lim[p]);
      float dt = bfround(expf(bf2f(logdt[p])));
      float ar = dt * lr, ai = dt * li;
      float e = expf(ar);
      lam = f32x2{e * cosf(ai), e * sinf(ai)};
      float nr = lam.x - 1.0f, ni = lam.y;
      float den = lr * lr + li * li;
      cf = f32x2{(nr * lr + ni * li) / den, (ni * lr - nr * li) / den};
      cd a{(double)lam.x, (double)lam.y};
      a = cmul(a, a); a = cmul(a, a); a = cmul(a, a);
      cd a8 = a, a16 = cmul(a8, a8), a24 = cmul(a16, a8), a32 = cmul(a16, a16), a64 = cmul(a32, a32), a96 = cmul(a64, a32);
      cd mu = cmul(a64, a64);
      l8 = tof(a8); l16 = tof(a16); l24 = tof(a24); l32 = tof(a32);
      lq = fq == 0 ? f32x2{1.f, 0.f} : fq == 1 ? l32 : fq == 2 ? tof(a64) : tof(a96);
      cd mu2 = cmul(mu, mu), mu4 = cmul(mu2, mu2), mu8 = cmul(mu4, mu4);
#pragma unroll
      for (int i = 0; i < 4; ++i) {
        int e2 = c - 1 - fq - 4 * i;
        cd r{1.0, 0.0};
        if (e2 & 1) r = cmul(r, mu);
        if (e2 & 2) r = cmul(r, mu2);
        if (e2 & 4) r = cmul(r, mu4);
        if (e2 & 8) r = cmul(r, mu8);
        mu_[i] = e2 >= 0 ? tof(r) : f32x2{0.f, 0.f};
      }
    }
    asm volatile("" : "+v"(lam), "+v"(cf), "+v"(l8), "+v"(l16), "+v"(l24), "+v"(l32), "+v"(lq));
    asm volatile("" : "+v"(mu_[0]), "+v"(mu_[1]), "+v"(mu_[2]), "+v"(mu_[3]));
    f32x4 acc[8][2];
#pragma unroll
    for (int m = 0; m < 8; ++m) { acc[m][0] = f32x4{0, 0, 0, 0}; acc[m][1] = f32x4{0, 0, 0, 0}; }
    bf16x8 F0[10], F1[10];
    auto rd = [&](bf16x8* F, int base) {
#pragma unroll
      for (int nn = 0; nn < 2; ++nn) F[8 + nn] = *LDSP(bf16x8, base + 16384 + 4096 * w + 2048 * nn);
#pragma unroll
      for (int m = 0; m < 8; ++m) F[m] = *LDSP(bf16x8, base + 2048 * m);
    };
    auto mm = [&](bf16x8* F) {
#pragma unroll
      for (int m = 0; m < 8; ++m)
#pragma unroll
        for (int nn = 0; nn < 2; ++nn) acc[m][nn] = __builtin_amdgcn_mfma_f32_16x16x32_bf16(F[m], F[8 + nn], acc[m][nn], 0, 0, 0);
    };
    STAMP(1);
    BAR();
    STAMP(2);
    rd(F0, a0);
    SB0();
    rd(F1, a1);
    mm(F0);
    ILV(10, 16)
    WAIT_L0();
    for (int s = 1; s < S1; ++s) {
      int buf = (s & 3) * ST1;
      BAR();
      SB0();
      rd(F0, buf + a0);
      mm(F1);
      ILV(10, 16)
      rd(F1, buf + a1);
      mm(F0);
      ILV(10, 16)
      WAIT_L0();
    }
    mm(F1);
    MFMA_DRAIN();
    STAMP(3);
    f32x2 u[32];
#pragma unroll
    for (int m = 0; m < 8; ++m)
#pragma unroll
      for (int j = 0; j < 4; ++j) {
        float a = acc[m][0][j], bb = acc[m][1][j];
        f32x2 t = f32x2{-cf.y, cf.x} * f32x2{bb, bb};
        u[4 * m + j] = __builtin_elementwise_fma(cf, f32x2{a, a}, t);
      }
    f32x2 ag[4];
#pragma unroll
    for (int k = 0; k < 4; ++k) ag[k] = u[8 * k];
#pragma unroll
    for (int i = 1; i < 8; ++i)
#pragma unroll
      for (int k = 0; k < 4; ++k) ag[k] = cfma(lam, ag[k], u[8 * k + i]);
    f32x2 e1 = ag[0], e2 = cfma(l8, e1, ag[1]), e3 = cfma(l8, e2, ag[2]), la = cfma(l8, e3, ag[3]);
    f32x2 q[4];
#pragma unroll
    for (int k = 0; k < 4; ++k) q[k] = f32x2{__shfl(la.x, fr + 16 * k), __shfl(la.y, fr + 16 * k)};
    f32x2 E1 = q[0], E2 = cfma(l32, E1, q[1]), E3 = cfma(l32, E2, q[2]), AG = cfma(l32, E3, q[3]);
    if (fq == 0 && c < 15) {
      u64 bits = (u64)__float_as_uint(AG.x) | ((u64)__float_as_uint(AG.y) << 32);
      if (bits == ~0ull) bits = 0x7fc000007fc00000ull;
      __hip_atomic_store(&exch[c * P + p], bits, __ATOMIC_RELAXED, __HIP_MEMORY_SCOPE_AGENT);
    }
    f32x2 E = fq == 0 ? f32x2{0.f, 0.f} : fq == 1 ? E1 : fq == 2 ? E2 : E3;
    f32x2 car = {0.f, 0.f};
    STAMP(4);
    if (c > 0) {
      u64 v[4];
      for (unsigned spin = 0;; ++spin) {
        bool ok = true;
#pragma unroll
        for (int i = 0; i < 4; ++i) {
          int j = min(fq + 4 * i, c - 1);
          v[i] = __hip_atomic_load(&exch[j * P + p], __ATOMIC_RELAXED, __HIP_MEMORY_SCOPE_AGENT);
          ok &= (v[i] != ~0ull);
        }
        if (__all(ok) || spin > (1u << 22)) break;
        __builtin_amdgcn_s_sleep(1);
      }
#pragma unroll
      for (int i = 0; i < 4; ++i) {
        f32x2 vv = {__uint_as_float((unsigned)v[i]), __uint_as_float((unsigned)(v[i] >> 32))};
        if (fq + 4 * i < c) car = cfma(mu_[i], vv, car);
      }
      car.x += __shfl_xor(car.x, 16); car.y += __shfl_xor(car.y, 16);
      car.x += __shfl_xor(car.x, 32); car.y += __shfl_xor(car.y, 32);
    }
    STAMP(5);
    f32x2 H = cfma(lq, car, E);
    f32x2 hs[4] = {H, cfma(l8, H, e1), cfma(l16, H, e2), cfma(l24, H, e3)};
#pragma unroll
    for (int i = 0; i < 8; ++i)
#pragma unroll
      for (int k = 0; k < 4; ++k) { hs[k] = cfma(lam, hs[k], u[8 * k + i]); u[8 * k + i] = hs[k]; }
#pragma unroll
    for (int i = 0; i < 4; ++i) {
      u32x4 vr, vi;
#pragma unroll
      for (int j = 0; j < 4; ++j) {
        vr[j] = pack2(u[8 * i + 2 * j].x, u[8 * i + 2 * j + 1].x);
        vi[j] = pack2(-u[8 * i + 2 * j].y, -u[8 * i + 2 * j + 1].y);
      }
      *LDSP(u32x4, (16 * w + fr) * IMG_PITCH + 64 * fq + 16 * i) = vr;
      *LDSP(u32x4, (64 + 16 * w + fr) * IMG_PITCH + 64 * fq + 16 * i) = vi;
    }
    WAIT_L0();
    BAR();
    {
      const __amdgpu_buffer_rsrc_t hr = __builtin_amdgcn_make_buffer_rsrc((void*)h, 0, 2048 * 2048 * 2, 0x00020000);
      const int a = (lane >> 2) & 3, bb = lane & 3;
#pragma unroll
      for (int tb = 0; tb < 2; ++tb)
#pragma unroll
        for (int cq = 0; cq < 4; ++cq) {
          int chunk = 4 * cq + fq;
          int tcol = (32 * w + 16 * tb + 4 * bb) * 2;
          int r1 = 8 * chunk + ((fq & 1) ? 4 : 0) + a, r2 = 8 * chunk + ((fq & 1) ? 0 : 4) + a;
          s16x4 v1 = __builtin_amdgcn_ds_read_tr16_b64_v4i16(LDSP(s16x4, r1 * IMG_PITCH + tcol));
          s16x4 v2 = __builtin_amdgcn_ds_read_tr16_b64_v4i16(LDSP(s16x4, r2 * IMG_PITCH + tcol));
          s16x4 lo = (fq & 1) ? v2 : v1, hi = (fq & 1) ? v1 : v2;
          bf16x8 o = __builtin_shufflevector(lo, hi, 0, 1, 2, 3, 4, 5, 6, 7);
          int t = 128 * c + 32 * w + 16 * tb + fr;
          __builtin_amdgcn_raw_buffer_store_b128(__builtin_bit_cast(u32x4, o), hr, (t * 2048 + 128 * g + 8 * chunk) * 2, 0, 16);
        }
    }
    STAMP(6);
    WAIT_V(0);
    if (lane == 0) __hip_atomic_store(&flags[c * 64 + g * 4 + w], 1u, __ATOMIC_RELAXED, __HIP_MEMORY_SCOPE_AGENT);
    STAMP(7);
    BAR();
    BAR();
  }
  const int wd = wave & 1, wt = wave >> 1;
  u32x2 xs[2][4], ds[2];
#pragma unroll
  for (int dm = 0; dm < 2; ++dm) {
    int d = 64 * n + 32 * wd + 16 * dm + 4 * fq;
    ds[dm] = *(const u32x2*)(Dskip + d);
#pragma unroll
    for (int tn = 0; tn < 4; ++tn) xs[dm][tn] = *(const u32x2*)(x + (size_t)(128 * c + 64 * wt + 16 * tn + fr) * D + d);
  }
  f32x4 acc[2][4];
#pragma unroll
  for (int i = 0; i < 2; ++i)
#pragma unroll
    for (int j = 0; j < 4; ++j) acc[i][j] = f32x4{0, 0, 0, 0};
  bf16x8 F0[6], F1[6];
  auto rd = [&](bf16x8* F, int hbase, int cbase) {
#pragma unroll
    for (int i = 0; i < 2; ++i) F[i] = *LDSP(bf16x8, cbase + (32 * wd + 16 * i) * 128);
#pragma unroll
    for (int j = 0; j < 4; ++j) F[2 + j] = *LDSP(bf16x8, hbase + (64 * wt + 16 * j) * 128);
  };
  auto mm = [&](bf16x8* F) {
#pragma unroll
    for (int i = 0; i < 2; ++i)
#pragma unroll
      for (int j = 0; j < 4; ++j) acc[i][j] = __builtin_amdgcn_mfma_f32_16x16x32_bf16(F[i], F[2 + j], acc[i][j], 0, 0, 0);
  };
  BAR();
  STAMP(8);
  rd(F0, a0, cslot(0) + a0);
  SB0();
  rd(F1, a1, cslot(0) + a1);
  mm(F0);
  ILV(6, 8)
  WAIT_L0();
  int bi = 1;
  for (int s = 1; s < S2; ++s) {
    int hb = bi * 16384, cb = bi < 4 ? 131072 + bi * 8192 : 114688 + (bi - 4) * 8192;
    BAR();
    SB0();
    rd(F0, hb + a0, cb + a0);
    mm(F1);
    ILV(6, 8)
    rd(F1, hb + a1, cb + a1);
    mm(F0);
    ILV(6, 8)
    WAIT_L0();
    bi = bi == NS2 - 1 ? 0 : bi + 1;
  }
  mm(F1);
  MFMA_DRAIN();
  STAMP(9);
#pragma unroll
  for (int dm = 0; dm < 2; ++dm) {
    int d = 64 * n + 32 * wd + 16 * dm + 4 * fq;
    float dk[4] = {__uint_as_float(ds[dm][0] << 16), __uint_as_float(ds[dm][0] & 0xffff0000u),
                   __uint_as_float(ds[dm][1] << 16), __uint_as_float(ds[dm][1] & 0xffff0000u)};
#pragma unroll
    for (int tn = 0; tn < 4; ++tn) {
      float xv[4] = {__uint_as_float(xs[dm][tn][0] << 16), __uint_as_float(xs[dm][tn][0] & 0xffff0000u),
                     __uint_as_float(xs[dm][tn][1] << 16), __uint_as_float(xs[dm][tn][1] & 0xffff0000u)};
      f32x4 o;
#pragma unroll
      for (int j = 0; j < 4; ++j) o[j] = 2.0f * acc[dm][tn][j] + bfround(xv[j] * dk[j]);
      *(f32x4*)(y + (size_t)(128 * c + 64 * wt + 16 * tn + fr) * D + d) = o;
    }
  }
}

__global__ __launch_bounds__(256) void k0_init(u32x4* p) {
  unsigned v = blockIdx.x < 32 ? ~0u : 0u;
  p[blockIdx.x * 256 + threadIdx.x] = u32x4{v, v, v, v};
}

extern "C" void kernel_launch(void* const* d_in, const int* in_sizes, int n_in, void* d_out, int out_size,
                              void* d_ws, size_t ws_size, hipStream_t stream) {
  const u16* x = (const u16*)d_in[0];
  const u16* Lre = (const u16*)d_in[1];
  const u16* Lim = (const u16*)d_in[2];
  const u16* Bre = (const u16*)d_in[3];
  const u16* Bim = (const u16*)d_in[4];
  const u16* Cre = (const u16*)d_in[5];
  const u16* Cim = (const u16*)d_in[6];
  const u16* logdt = (const u16*)d_in[7];
  const u16* Dskip = (const u16*)d_in[8];
  float* out = (float*)d_out;
  char* ws = (char*)d_ws;
  u64* exch = (u64*)ws;
  unsigned* flags = (unsigned*)(ws + 131072);
  u16* h = (u16*)(ws + (1 << 20));
  (void)hipFuncSetAttribute((const void*)s5_fused, hipFuncAttributeMaxDynamicSharedMemorySize, LDS_BYTES);
  k0_init<<<33, 256, 0, stream>>>((u32x4*)exch);
  s5_fused<<<256, 512, LDS_BYTES, stream>>>(x, Bre, Bim, Cre, Cim, Lre, Lim, logdt, Dskip, h, exch, flags, out TSPASS);
}
```

```cpp
#include <hip/hip_runtime.h>
#include <hip/hip_bf16.h>
#include <stdint.h>

typedef __attribute__((ext_vector_type(8))) short bf16x8;
typedef __attribute__((ext_vector_type(4))) short s16x4;
typedef __attribute__((ext_vector_type(4))) float f32x4;
typedef __attribute__((ext_vector_type(2))) float f32x2;
typedef __attribute__((ext_vector_type(4))) unsigned u32x4;
typedef __attribute__((ext_vector_type(2))) unsigned u32x2;
typedef unsigned short u16;
typedef unsigned long long u64;
#define LDSP(T, off) ((__attribute__((address_space(3))) T*)(smem + (off)))

#define STAMP(i)
#define LSTAMP(i)
#define TSARG
#define TSPASS

static constexpr int D = 1024, P = 1024;

__device__ __forceinline__ float bf2f(u16 v) { return __uint_as_float(((unsigned)v) << 16); }
__device__ __forceinline__ float bfround(float f) { return __bfloat162float(__float2bfloat16(f)); }
__device__ __forceinline__ unsigned pack2(float a, float b) {
  __hip_bfloat16 x = __float2bfloat16(a), y = __float2bfloat16(b);
  return (unsigned)(*reinterpret_cast<u16*>(&x)) | ((unsigned)(*reinterpret_cast<u16*>(&y)) << 16);
}
#define WAIT_V(n) asm volatile("s_waitcnt vmcnt(%0)" ::"i"(n) : "memory")
#define WAIT_L0() asm volatile("s_waitcnt lgkmcnt(0)" ::: "memory")
#define BAR() asm volatile("s_barrier" ::: "memory")
#define SB0() __builtin_amdgcn_sched_barrier(0)
#define MFMA_DRAIN() do { SB0(); asm volatile("s_nop 15\n\ts_nop 15"); SB0(); } while (0)
#define ILV(NR, NM)                                                        \
  _Pragma("unroll") for (int i_ = 0; i_ < NR; ++i_) {                       \
    __builtin_amdgcn_sched_group_barrier(0x8, 1, 0);                        \
    __builtin_amdgcn_sched_group_barrier(0x100, 1, 0);                      \
  }                                                                         \
  __builtin_amdgcn_sched_group_barrier(0x8, NM - NR, 0);                    \
  SB0();
#define GLDS(gp, lo) __builtin_amdgcn_global_load_lds((const unsigned*)(gp), LDSP(unsigned, (lo)), 16, 0, 0)
#define GLDS_SC1(gp, lo) __builtin_amdgcn_global_load_lds((const unsigned*)(gp), LDSP(unsigned, (lo)), 16, 0, 16)

struct cd { double r, i; };
__device__ __forceinline__ cd cmul(cd a, cd b) { return cd{a.r * b.r - a.i * b.i, a.r * b.i + a.i * b.r}; }
__device__ __forceinline__ f32x2 tof(cd a) { return f32x2{(float)a.r, (float)a.i}; }
__device__ __forceinline__ f32x2 cfma(f32x2 a, f32x2 b, f32x2 c) {
  f32x2 t = __builtin_elementwise_fma(f32x2{a.x, a.x}, b, c);
  return __builtin_elementwise_fma(f32x2{-a.y, a.y}, f32x2{b.y, b.x}, t);
}

static constexpr int S1 = 16, ST1 = 32768;
static constexpr int S2 = 32, NS2 = 6;
static constexpr int LDS_BYTES = 163840;
static constexpr int IMG_PITCH = 288;
__device__ __forceinline__ constexpr int cslot(int i) { return i < 4 ? 131072 + i * 8192 : 114688 + (i - 4) * 8192; }

__global__ __launch_bounds__(512, 2) void s5_fused(const u16* __restrict__ x, const u16* __restrict__ Bre,
                                                   const u16* __restrict__ Bim, const u16* __restrict__ Cre,
                                                   const u16* __restrict__ Cim, const u16* __restrict__ Lre,
                                                   const u16* __restrict__ Lim, const u16* __restrict__ logdt,
                                                   const u16* __restrict__ Dskip, u16* h, u64* exch,
                                                   unsigned* flags, float* __restrict__ y TSARG) {
  extern __shared__ __attribute__((aligned(16))) char smem[];
  const int b = blockIdx.x, xcd = b & 7, idx = b >> 3;
  const int c = 4 * (xcd & 3) + (idx & 3), g = 8 * (xcd >> 2) + (idx >> 2);
  const int n = g;
  const int wave = __builtin_amdgcn_readfirstlane(threadIdx.x >> 6), lane = threadIdx.x & 63;
  if (wave >= 4) {
    const int lw = wave - 4;
    const int koff2 = 0;
    int offC[2];
#pragma unroll
    for (int i = 0; i < 2; ++i) {
      int rho = 16 * lw + 8 * i + (lane >> 3);
      int chunk = (lane & 7) ^ ((rho >> 1) & 7);
      offC[i] = (64 * n + rho) * P + chunk * 8;
    }
    auto issueC = [&](int s0, int slot_off) {
      int s = (s0 + koff2) & 31;
      const u16* Cb = ((s & 1) ? Cim : Cre) + (s >> 1) * 64;
#pragma unroll
      for (int i = 0; i < 2; ++i) GLDS(Cb + offC[i], slot_off + (2 * lw + i) * 1024);
    };
    {
      const u16* src[8];
#pragma unroll
      for (int i = 0; i < 4; ++i) {
        int rho = 32 * lw + 8 * i + (lane >> 3);
        int chunk = (lane & 7) ^ ((rho >> 1) & 7);
        int m = rho >> 4, i16 = rho & 15;
        int tau = 32 * (i16 >> 2) + 4 * m + (i16 & 3);
        src[i] = x + (size_t)(128 * c + tau) * D + chunk * 8;
        int p = 64 * g + 16 * (rho >> 5) + (rho & 15);
        src[4 + i] = ((rho & 16) ? Bim : Bre) + (size_t)p * D + chunk * 8;
      }
      const int koff = 2 * (idx >> 2) + 4 * (idx & 3);
      auto issue = [&](int s0) {
        int buf = (s0 & 3) * ST1;
        int s = (s0 + koff) & 15;
#pragma unroll
        for (int i = 0; i < 4; ++i) GLDS(src[i] + s * 64, buf + (4 * lw + i) * 1024);
#pragma unroll
        for (int i = 0; i < 4; ++i) GLDS(src[4 + i] + s * 64, buf + 16384 + (4 * lw + i) * 1024);
      };
      issue(0); issue(1); issue(2);
      for (int s = 0; s < 6; ++s) { WAIT_V(16); BAR(); issue(s + 3); }
      WAIT_V(16); BAR(); issue(9);
      {
        const char* p1 = (const char*)Cre + b * 8192 + lw * 2048 + lane * 16;
        const char* p2 = (const char*)Cim + b * 8192 + lw * 2048 + lane * 16;
        GLDS(p1, 131072 + lw * 1024); GLDS(p1 + 1024, 131072 + lw * 1024);
        GLDS(p2, 131072 + lw * 1024); GLDS(p2 + 1024, 131072 + lw * 1024);
      }
      WAIT_V(20); BAR(); issue(10);
      WAIT_V(20); BAR(); issue(11);
      WAIT_V(20); BAR(); issue(12);
      WAIT_V(16); BAR(); issue(13);
      WAIT_V(16); BAR(); issue(14);
      WAIT_V(16); BAR(); issue(15);
      issueC(0, cslot(0)); issueC(1, cslot(1)); issueC(2, cslot(2)); issueC(3, cslot(3));
      WAIT_V(24); BAR();
      WAIT_V(16); BAR();
      WAIT_V(8); BAR();
    }
    const u16* srcH[4];
#pragma unroll
    for (int i = 0; i < 4; ++i) {
      int rho = 32 * lw + 8 * i + (lane >> 3);
      int chunk = (lane & 7) ^ ((rho >> 1) & 7);
      srcH[i] = h + (size_t)(128 * c + rho) * 2048 + chunk * 8;
    }
    auto issueH = [&](int s0, int bi) {
      int s = (s0 + koff2) & 31;
#pragma unroll
      for (int i = 0; i < 4; ++i) GLDS_SC1(srcH[i] + s * 64, bi * 16384 + (4 * lw + i) * 1024);
    };
    BAR();
    BAR();
    if (lw == 0) {
      for (unsigned spin = 0;; ++spin) {
        unsigned v = __hip_atomic_load(&flags[c * 64 + lane], __ATOMIC_RELAXED, __HIP_MEMORY_SCOPE_AGENT);
        if (__all(v == 1u) || spin > (1u << 22)) break;
        __builtin_amdgcn_s_sleep(4);
      }
    }
    LSTAMP(12);
    BAR();
    issueH(0, 0); issueH(1, 1); issueH(2, 2); issueH(3, 3);
    issueH(4, 4); issueC(4, cslot(4));
    WAIT_V(18); BAR(); issueH(5, 5); issueC(5, cslot(5));
    WAIT_V(20); BAR(); issueH(6, 0); issueC(6, cslot(0));
    WAIT_V(22); BAR(); issueH(7, 1); issueC(7, cslot(1));
    WAIT_V(24); BAR(); issueH(8, 2); issueC(8, cslot(2));
    for (int s = 4; s < 22; s += 6) {
      WAIT_V(24); BAR(); issueH(s + 5, 3); issueC(s + 5, cslot(3));
      WAIT_V(24); BAR(); issueH(s + 6, 4); issueC(s + 6, cslot(4));
      WAIT_V(24); BAR(); issueH(s + 7, 5); issueC(s + 7, cslot(5));
      WAIT_V(24); BAR(); issueH(s + 8, 0); issueC(s + 8, cslot(0));
      WAIT_V(24); BAR(); issueH(s + 9, 1); issueC(s + 9, cslot(1));
      WAIT_V(24); BAR(); issueH(s + 10, 2); issueC(s + 10, cslot(2));
    }
    WAIT_V(24); BAR(); issueH(27, 3); issueC(27, cslot(3));
    WAIT_V(24); BAR(); issueH(28, 4); issueC(28, cslot(4));
    WAIT_V(24); BAR(); issueH(29, 5); issueC(29, cslot(5));
    WAIT_V(24); BAR(); issueH(30, 0); issueC(30, cslot(0));
    WAIT_V(24); BAR(); issueH(31, 1); issueC(31, cslot(1));
    WAIT_V(24); BAR();
    WAIT_V(18); BAR();
    WAIT_V(12); BAR();
    WAIT_V(6); BAR();
    WAIT_V(0); BAR();
    return;
  }
  STAMP(0);
  unsigned tlb_;
  {
    const char* pt = lane == 0 ? (const char*)(h + (size_t)(128 * c) * 2048)
                   : lane == 1 ? (const char*)(y + (size_t)(128 * c) * D)
                   : lane == 2 ? (const char*)(exch + c * P)
                   : lane == 3 ? (const char*)(Cre + (size_t)(64 * n) * P)
                   : lane == 4 ? (const char*)(Cim + (size_t)(64 * n) * P)
                   : lane == 5 ? (const char*)Dskip
                   : (const char*)(flags + c * 64);
    tlb_ = __builtin_nontemporal_load((const unsigned*)pt);
  }
  const int w = wave, fr = lane & 15, fq = lane >> 4;
  const int c0 = fq ^ (fr >> 1);
  const int a0 = fr * 128 + c0 * 16, a1 = fr * 128 + (c0 ^ 4) * 16;
  {
    const int p = 64 * g + 16 * w + fr;
    f32x2 lam, cf, l8, l16, l24, l32, lq, mu_[4];
    {
      float lr = bf2f(Lre[p]), li = bf2f(Lim[p]);
      float dt = bfround(expf(bf2f(logdt[p])));
      float ar = dt * lr, ai = dt * li;
      float e = expf(ar);
      lam = f32x2{e * cosf(ai), e * sinf(ai)};
      float nr = lam.x - 1.0f, ni = lam.y;
      float den = lr * lr + li * li;
      cf = f32x2{(nr * lr + ni * li) / den, (ni * lr - nr * li) / den};
      cd a{(double)lam.x, (double)lam.y};
      a = cmul(a, a); a = cmul(a, a); a = cmul(a, a);
      cd a8 = a, a16 = cmul(a8, a8), a24 = cmul(a16, a8), a32 = cmul(a16, a16), a64 = cmul(a32, a32), a96 = cmul(a64, a32);
      cd mu = cmul(a64, a64);
      l8 = tof(a8); l16 = tof(a16); l24 = tof(a24); l32 = tof(a32);
      lq = fq == 0 ? f32x2{1.f, 0.f} : fq == 1 ? l32 : fq == 2 ? tof(a64) : tof(a96);
      cd mu2 = cmul(mu, mu), mu4 = cmul(mu2, mu2), mu8 = cmul(mu4, mu4);
#pragma unroll
      for (int i = 0; i < 4; ++i) {
        int e2 = c - 1 - fq - 4 * i;
        cd r{1.0, 0.0};
        if (e2 & 1) r = cmul(r, mu);
        if (e2 & 2) r = cmul(r, mu2);
        if (e2 & 4) r = cmul(r, mu4);
        if (e2 & 8) r = cmul(r, mu8);
        mu_[i] = e2 >= 0 ? tof(r) : f32x2{0.f, 0.f};
      }
    }
    asm volatile("" : "+v"(lam), "+v"(cf), "+v"(l8), "+v"(l16), "+v"(l24), "+v"(l32), "+v"(lq));
    asm volatile("" : "+v"(mu_[0]), "+v"(mu_[1]), "+v"(mu_[2]), "+v"(mu_[3]));
    f32x4 acc[8][2];
#pragma unroll
    for (int m = 0; m < 8; ++m) { acc[m][0] = f32x4{0, 0, 0, 0}; acc[m][1] = f32x4{0, 0, 0, 0}; }
    bf16x8 F0[10], F1[10];
    auto rd = [&](bf16x8* F, int base) {
#pragma unroll
      for (int nn = 0; nn < 2; ++nn) F[8 + nn] = *LDSP(bf16x8, base + 16384 + 4096 * w + 2048 * nn);
#pragma unroll
      for (int m = 0; m < 8; ++m) F[m] = *LDSP(bf16x8, base + 2048 * m);
    };
    auto mm = [&](bf16x8* F) {
#pragma unroll
      for (int m = 0; m < 8; ++m)
#pragma unroll
        for (int nn = 0; nn < 2; ++nn) acc[m][nn] = __builtin_amdgcn_mfma_f32_16x16x32_bf16(F[m], F[8 + nn], acc[m][nn], 0, 0, 0);
    };
    STAMP(1);
    BAR();
    STAMP(2);
    rd(F0, a0);
    SB0();
    rd(F1, a1);
    mm(F0);
    ILV(10, 16)
    WAIT_L0();
    for (int s = 1; s < S1; ++s) {
      int buf = (s & 3) * ST1;
      BAR();
      SB0();
      rd(F0, buf + a0);
      mm(F1);
      ILV(10, 16)
      rd(F1, buf + a1);
      mm(F0);
      ILV(10, 16)
      WAIT_L0();
    }
    mm(F1);
    MFMA_DRAIN();
    asm volatile("" ::"v"(tlb_));
    STAMP(3);
    f32x2 u[32];
#pragma unroll
    for (int m = 0; m < 8; ++m)
#pragma unroll
      for (int j = 0; j < 4; ++j) {
        float a = acc[m][0][j], bb = acc[m][1][j];
        f32x2 t = f32x2{-cf.y, cf.x} * f32x2{bb, bb};
        u[4 * m + j] = __builtin_elementwise_fma(cf, f32x2{a, a}, t);
      }
    f32x2 ag[4];
#pragma unroll
    for (int k = 0; k < 4; ++k) ag[k] = u[8 * k];
#pragma unroll
    for (int i = 1; i < 8; ++i)
#pragma unroll
      for (int k = 0; k < 4; ++k) ag[k] = cfma(lam, ag[k], u[8 * k + i]);
    f32x2 e1 = ag[0], e2 = cfma(l8, e1, ag[1]), e3 = cfma(l8, e2, ag[2]), la = cfma(l8, e3, ag[3]);
    f32x2 q[4];
#pragma unroll
    for (int k = 0; k < 4; ++k) q[k] = f32x2{__shfl(la.x, fr + 16 * k), __shfl(la.y, fr + 16 * k)};
    f32x2 E1 = q[0], E2 = cfma(l32, E1, q[1]), E3 = cfma(l32, E2, q[2]), AG = cfma(l32, E3, q[3]);
    if (fq == 0 && c < 15) {
      u64 bits = (u64)__float_as_uint(AG.x) | ((u64)__float_as_uint(AG.y) << 32);
      if (bits == ~0ull) bits = 0x7fc000007fc00000ull;
      __hip_atomic_store(&exch[c * P + p], bits, __ATOMIC_RELAXED, __HIP_MEMORY_SCOPE_AGENT);
    }
    f32x2 E = fq == 0 ? f32x2{0.f, 0.f} : fq == 1 ? E1 : fq == 2 ? E2 : E3;
    f32x2 car = {0.f, 0.f};
    STAMP(4);
    if (c > 0) {
      u64 v[4];
      for (unsigned spin = 0;; ++spin) {
        bool ok = true;
#pragma unroll
        for (int i = 0; i < 4; ++i) {
          int j = min(fq + 4 * i, c - 1);
          v[i] = __hip_atomic_load(&exch[j * P + p], __ATOMIC_RELAXED, __HIP_MEMORY_SCOPE_AGENT);
          ok &= (v[i] != ~0ull);
        }
        if (__all(ok) || spin > (1u << 22)) break;
        __builtin_amdgcn_s_sleep(1);
      }
#pragma unroll
      for (int i = 0; i < 4; ++i) {
        f32x2 vv = {__uint_as_float((unsigned)v[i]), __uint_as_float((unsigned)(v[i] >> 32))};
        if (fq + 4 * i < c) car = cfma(mu_[i], vv, car);
      }
      car.x += __shfl_xor(car.x, 16); car.y += __shfl_xor(car.y, 16);
      car.x += __shfl_xor(car.x, 32); car.y += __shfl_xor(car.y, 32);
    }
    STAMP(5);
    f32x2 H = cfma(lq, car, E);
    f32x2 hs[4] = {H, cfma(l8, H, e1), cfma(l16, H, e2), cfma(l24, H, e3)};
#pragma unroll
    for (int i = 0; i < 8; ++i)
#pragma unroll
      for (int k = 0; k < 4; ++k) { hs[k] = cfma(lam, hs[k], u[8 * k + i]); u[8 * k + i] = hs[k]; }
#pragma unroll
    for (int i = 0; i < 4; ++i) {
      u32x4 vr, vi;
#pragma unroll
      for (int j = 0; j < 4; ++j) {
        vr[j] = pack2(u[8 * i + 2 * j].x, u[8 * i + 2 * j + 1].x);
        vi[j] = pack2(-u[8 * i + 2 * j].y, -u[8 * i + 2 * j + 1].y);
      }
      *LDSP(u32x4, (16 * w + fr) * IMG_PITCH + 64 * fq + 16 * i) = vr;
      *LDSP(u32x4, (64 + 16 * w + fr) * IMG_PITCH + 64 * fq + 16 * i) = vi;
    }
    WAIT_L0();
    BAR();
    {
      const __amdgpu_buffer_rsrc_t hr = __builtin_amdgcn_make_buffer_rsrc((void*)h, 0, 2048 * 2048 * 2, 0x00020000);
      const int a = (lane >> 2) & 3, bb = lane & 3;
#pragma unroll
      for (int tb = 0; tb < 2; ++tb)
#pragma unroll
        for (int cq = 0; cq < 4; ++cq) {
          int chunk = 4 * cq + fq;
          int tcol = (32 * w + 16 * tb + 4 * bb) * 2;
          int r1 = 8 * chunk + ((fq & 1) ? 4 : 0) + a, r2 = 8 * chunk + ((fq & 1) ? 0 : 4) + a;
          s16x4 v1 = __builtin_amdgcn_ds_read_tr16_b64_v4i16(LDSP(s16x4, r1 * IMG_PITCH + tcol));
          s16x4 v2 = __builtin_amdgcn_ds_read_tr16_b64_v4i16(LDSP(s16x4, r2 * IMG_PITCH + tcol));
          s16x4 lo = (fq & 1) ? v2 : v1, hi = (fq & 1) ? v1 : v2;
          bf16x8 o = __builtin_shufflevector(lo, hi, 0, 1, 2, 3, 4, 5, 6, 7);
          int t = 128 * c + 32 * w + 16 * tb + fr;
          __builtin_amdgcn_raw_buffer_store_b128(__builtin_bit_cast(u32x4, o), hr, (t * 2048 + 128 * g + 8 * chunk) * 2, 0, 16);
        }
    }
    STAMP(6);
    WAIT_V(0);
    if (lane == 0) __hip_atomic_store(&flags[c * 64 + g * 4 + w], 1u, __ATOMIC_RELAXED, __HIP_MEMORY_SCOPE_AGENT);
    STAMP(7);
    BAR();
    BAR();
  }
  const int wd = wave & 1, wt = wave >> 1;
  u32x2 xs[2][4], ds[2];
#pragma unroll
  for (int dm = 0; dm < 2; ++dm) {
    int d = 64 * n + 32 * wd + 16 * dm + 4 * fq;
    ds[dm] = *(const u32x2*)(Dskip + d);
#pragma unroll
    for (int tn = 0; tn < 4; ++tn) xs[dm][tn] = *(const u32x2*)(x + (size_t)(128 * c + 64 * wt + 16 * tn + fr) * D + d);
  }
  f32x4 acc[2][4];
#pragma unroll
  for (int i = 0; i < 2; ++i)
#pragma unroll
    for (int j = 0; j < 4; ++j) acc[i][j] = f32x4{0, 0, 0, 0};
  bf16x8 F0[6], F1[6];
  auto rd = [&](bf16x8* F, int hbase, int cbase) {
#pragma unroll
    for (int i = 0; i < 2; ++i) F[i] = *LDSP(bf16x8, cbase + (32 * wd + 16 * i) * 128);
#pragma unroll
    for (int j = 0; j < 4; ++j) F[2 + j] = *LDSP(bf16x8, hbase + (64 * wt + 16 * j) * 128);
  };
  auto mm = [&](bf16x8* F) {
#pragma unroll
    for (int i = 0; i < 2; ++i)
#pragma unroll
      for (int j = 0; j < 4; ++j) acc[i][j] = __builtin_amdgcn_mfma_f32_16x16x32_bf16(F[i], F[2 + j], acc[i][j], 0, 0, 0);
  };
  BAR();
  STAMP(8);
  rd(F0, a0, cslot(0) + a0);
  SB0();
  rd(F1, a1, cslot(0) + a1);
  mm(F0);
  ILV(6, 8)
  WAIT_L0();
  int bi = 1;
  for (int s = 1; s < S2; ++s) {
    int hb = bi * 16384, cb = bi < 4 ? 131072 + bi * 8192 : 114688 + (bi - 4) * 8192;
    BAR();
    SB0();
    rd(F0, hb + a0, cb + a0);
    mm(F1);
    ILV(6, 8)
    rd(F1, hb + a1, cb + a1);
    mm(F0);
    ILV(6, 8)
    WAIT_L0();
    bi = bi == NS2 - 1 ? 0 : bi + 1;
  }
  mm(F1);
  MFMA_DRAIN();
  STAMP(9);
#pragma unroll
  for (int dm = 0; dm < 2; ++dm) {
    int d = 64 * n + 32 * wd + 16 * dm + 4 * fq;
    float dk[4] = {__uint_as_float(ds[dm][0] << 16), __uint_as_float(ds[dm][0] & 0xffff0000u),
                   __uint_as_float(ds[dm][1] << 16), __uint_as_float(ds[dm][1] & 0xffff0000u)};
#pragma unroll
    for (int tn = 0; tn < 4; ++tn) {
      float xv[4] = {__uint_as_float(xs[dm][tn][0] << 16), __uint_as_float(xs[dm][tn][0] & 0xffff0000u),
                     __uint_as_float(xs[dm][tn][1] << 16), __uint_as_float(xs[dm][tn][1] & 0xffff0000u)};
      f32x4 o;
#pragma unroll
      for (int j = 0; j < 4; ++j) o[j] = 2.0f * acc[dm][tn][j] + bfround(xv[j] * dk[j]);
      *(f32x4*)(y + (size_t)(128 * c + 64 * wt + 16 * tn + fr) * D + d) = o;
    }
  }
}

__global__ __launch_bounds__(256) void k0_init(u32x4* p) {
  unsigned v = blockIdx.x < 32 ? ~0u : 0u;
  p[blockIdx.x * 256 + threadIdx.x] = u32x4{v, v, v, v};
}

extern "C" void kernel_launch(void* const* d_in, const int* in_sizes, int n_in, void* d_out, int out_size,
                              void* d_ws, size_t ws_size, hipStream_t stream) {
  const u16* x = (const u16*)d_in[0];
  const u16* Lre = (const u16*)d_in[1];
  const u16* Lim = (const u16*)d_in[2];
  const u16* Bre = (const u16*)d_in[3];
  const u16* Bim = (const u16*)d_in[4];
  const u16* Cre = (const u16*)d_in[5];
  const u16* Cim = (const u16*)d_in[6];
  const u16* logdt = (const u16*)d_in[7];
  const u16* Dskip = (const u16*)d_in[8];
  float* out = (float*)d_out;
  char* ws = (char*)d_ws;
  u64* exch = (u64*)ws;
  unsigned* flags = (unsigned*)(ws + 131072);
  u16* h = (u16*)(ws + (1 << 20));
  (void)hipFuncSetAttribute((const void*)s5_fused, hipFuncAttributeMaxDynamicSharedMemorySize, LDS_BYTES);
  k0_init<<<33, 256, 0, stream>>>((u32x4*)exch);
  s5_fused<<<256, 512, LDS_BYTES, stream>>>(x, Bre, Bim, Cre, Cim, Lre, Lim, logdt, Dskip, h, exch, flags, out TSPASS);
}
```

```cpp
#include <hip/hip_runtime.h>
#include <hip/hip_bf16.h>
#include <stdint.h>

typedef __attribute__((ext_vector_type(8))) short bf16x8;
typedef __attribute__((ext_vector_type(4))) short s16x4;
typedef __attribute__((ext_vector_type(4))) float f32x4;
typedef __attribute__((ext_vector_type(2))) float f32x2;
typedef __attribute__((ext_vector_type(4))) unsigned u32x4;
typedef __attribute__((ext_vector_type(2))) unsigned u32x2;
typedef unsigned short u16;
typedef unsigned long long u64;
#define LDSP(T, off) ((__attribute__((address_space(3))) T*)(smem + (off)))

#define STAMP(i)
#define LSTAMP(i)
#define TSARG
#define TSPASS

static constexpr int D = 1024, P = 1024;

__device__ __forceinline__ float bf2f(u16 v) { return __uint_as_float(((unsigned)v) << 16); }
__device__ __forceinline__ float bfround(float f) { return __bfloat162float(__float2bfloat16(f)); }
__device__ __forceinline__ unsigned pack2(float a, float b) {
  __hip_bfloat16 x = __float2bfloat16(a), y = __float2bfloat16(b);
  return (unsigned)(*reinterpret_cast<u16*>(&x)) | ((unsigned)(*reinterpret_cast<u16*>(&y)) << 16);
}
#define WAIT_V(n) asm volatile("s_waitcnt vmcnt(%0)" ::"i"(n) : "memory")
#define WAIT_L0() asm volatile("s_waitcnt lgkmcnt(0)" ::: "memory")
#define BAR() asm volatile("s_barrier" ::: "memory")
#define SB0() __builtin_amdgcn_sched_barrier(0)
#define MFMA_DRAIN() do { SB0(); asm volatile("s_nop 15\n\ts_nop 15"); SB0(); } while (0)
#define ILV(NR, NM)                                                        \
  _Pragma("unroll") for (int i_ = 0; i_ < NR; ++i_) {                       \
    __builtin_amdgcn_sched_group_barrier(0x8, 1, 0);                        \
    __builtin_amdgcn_sched_group_barrier(0x100, 1, 0);                      \
  }                                                                         \
  __builtin_amdgcn_sched_group_barrier(0x8, NM - NR, 0);                    \
  SB0();
#define GLDS(gp, lo) __builtin_amdgcn_global_load_lds((const unsigned*)(gp), LDSP(unsigned, (lo)), 16, 0, 0)
#define GLDS_SC1(gp, lo) __builtin_amdgcn_global_load_lds((const unsigned*)(gp), LDSP(unsigned, (lo)), 16, 0, 16)

extern "C" __device__ unsigned long long s5_dispatch_id() __asm("llvm.amdgcn.dispatch.id");
__device__ __forceinline__ u32x2 launch_tag() {
  u64 z = s5_dispatch_id() * 0x9E3779B97F4A7C15ull + ((u64)__builtin_amdgcn_dispatch_ptr() >> 6) * 0xD1B54A32D192ED03ull + 0x2545F4914F6CDD1Dull;
  z = (z ^ (z >> 30)) * 0xBF58476D1CE4E5B9ull;
  z = (z ^ (z >> 27)) * 0x94D049BB133111EBull;
  z ^= z >> 31;
  unsigned a = (unsigned)z, b = (unsigned)(z >> 32);
  a = (a & 0xFFFF00FFu) | ((~a & 0xFFu) << 8);
  b = (b & 0xFFFF00FFu) | ((~b & 0xFFu) << 8);
  return u32x2{a, b};
}

struct cd { double r, i; };
__device__ __forceinline__ cd cmul(cd a, cd b) { return cd{a.r * b.r - a.i * b.i, a.r * b.i + a.i * b.r}; }
__device__ __forceinline__ f32x2 tof(cd a) { return f32x2{(float)a.r, (float)a.i}; }
__device__ __forceinline__ f32x2 cfma(f32x2 a, f32x2 b, f32x2 c) {
  f32x2 t = __builtin_elementwise_fma(f32x2{a.x, a.x}, b, c);
  return __builtin_elementwise_fma(f32x2{-a.y, a.y}, f32x2{b.y, b.x}, t);
}

static constexpr int S1 = 16, ST1 = 32768;
static constexpr int S2 = 32, NS2 = 6;
static constexpr int LDS_BYTES = 163840;
static constexpr int IMG_PITCH = 288;
__device__ __forceinline__ constexpr int cslot(int i) { return i < 4 ? 131072 + i * 8192 : 114688 + (i - 4) * 8192; }

__global__ __launch_bounds__(512, 2) void s5_fused(const u16* __restrict__ x, const u16* __restrict__ Bre,
                                                   const u16* __restrict__ Bim, const u16* __restrict__ Cre,
                                                   const u16* __restrict__ Cim, const u16* __restrict__ Lre,
                                                   const u16* __restrict__ Lim, const u16* __restrict__ logdt,
                                                   const u16* __restrict__ Dskip, u16* h, u32x4* exch,
                                                   u64* flags, float* __restrict__ y TSARG) {
  extern __shared__ __attribute__((aligned(16))) char smem[];
  const int b = blockIdx.x, xcd = b & 7, idx = b >> 3;
  const int c = 4 * (xcd & 3) + (idx & 3), g = 8 * (xcd >> 2) + (idx >> 2);
  const int n = g;
  const int wave = __builtin_amdgcn_readfirstlane(threadIdx.x >> 6), lane = threadIdx.x & 63;
  const u32x2 tag = launch_tag();
  const u64 tag64 = (u64)tag.x | ((u64)tag.y << 32);
  if (wave >= 4) {
    const int lw = wave - 4;
    const int koff2 = 0;
    int offC[2];
#pragma unroll
    for (int i = 0; i < 2; ++i) {
      int rho = 16 * lw + 8 * i + (lane >> 3);
      int chunk = (lane & 7) ^ ((rho >> 1) & 7);
      offC[i] = (64 * n + rho) * P + chunk * 8;
    }
    auto issueC = [&](int s0, int slot_off) {
      int s = (s0 + koff2) & 31;
      const u16* Cb = ((s & 1) ? Cim : Cre) + (s >> 1) * 64;
#pragma unroll
      for (int i = 0; i < 2; ++i) GLDS(Cb + offC[i], slot_off + (2 * lw + i) * 1024);
    };
    {
      const u16* src[8];
#pragma unroll
      for (int i = 0; i < 4; ++i) {
        int rho = 32 * lw + 8 * i + (lane >> 3);
        int chunk = (lane & 7) ^ ((rho >> 1) & 7);
        int m = rho >> 4, i16 = rho & 15;
        int tau = 32 * (i16 >> 2) + 4 * m + (i16 & 3);
        src[i] = x + (size_t)(128 * c + tau) * D + chunk * 8;
        int p = 64 * g + 16 * (rho >> 5) + (rho & 15);
        src[4 + i] = ((rho & 16) ? Bim : Bre) + (size_t)p * D + chunk * 8;
      }
      const int koff = 2 * (idx >> 2) + 4 * (idx & 3);
      auto issue = [&](int s0) {
        int buf = (s0 & 3) * ST1;
        int s = (s0 + koff) & 15;
#pragma unroll
        for (int i = 0; i < 4; ++i) GLDS(src[i] + s * 64, buf + (4 * lw + i) * 1024);
#pragma unroll
        for (int i = 0; i < 4; ++i) GLDS(src[4 + i] + s * 64, buf + 16384 + (4 * lw + i) * 1024);
      };
      issue(0); issue(1); issue(2);
      for (int s = 0; s < 6; ++s) { WAIT_V(16); BAR(); issue(s + 3); }
      WAIT_V(16); BAR(); issue(9);
      {
        const char* p1 = (const char*)Cre + b * 8192 + lw * 2048 + lane * 16;
        const char* p2 = (const char*)Cim + b * 8192 + lw * 2048 + lane * 16;
        GLDS(p1, 131072 + lw * 1024); GLDS(p1 + 1024, 131072 + lw * 1024);
        GLDS(p2, 131072 + lw * 1024); GLDS(p2 + 1024, 131072 + lw * 1024);
      }
      WAIT_V(20); BAR(); issue(10);
      WAIT_V(20); BAR(); issue(11);
      WAIT_V(20); BAR(); issue(12);
      WAIT_V(16); BAR(); issue(13);
      WAIT_V(16); BAR(); issue(14);
      WAIT_V(16); BAR(); issue(15);
      issueC(0, cslot(0)); issueC(1, cslot(1)); issueC(2, cslot(2)); issueC(3, cslot(3));
      WAIT_V(24); BAR();
      WAIT_V(16); BAR();
      WAIT_V(8); BAR();
    }
    const u16* srcH[4];
#pragma unroll
    for (int i = 0; i < 4; ++i) {
      int rho = 32 * lw + 8 * i + (lane >> 3);
      int chunk = (lane & 7) ^ ((rho >> 1) & 7);
      srcH[i] = h + (size_t)(128 * c + rho) * 2048 + chunk * 8;
    }
    auto issueH = [&](int s0, int bi) {
      int s = (s0 + koff2) & 31;
#pragma unroll
      for (int i = 0; i < 4; ++i) GLDS_SC1(srcH[i] + s * 64, bi * 16384 + (4 * lw + i) * 1024);
    };
    BAR();
    BAR();
    if (lw == 0) {
      for (unsigned spin = 0;; ++spin) {
        u64 v = __hip_atomic_load(&flags[c * 64 + lane], __ATOMIC_RELAXED, __HIP_MEMORY_SCOPE_AGENT);
        if (__all(v == tag64) || spin > (1u << 22)) break;
        __builtin_amdgcn_s_sleep(4);
      }
    }
    LSTAMP(12);
    BAR();
    issueH(0, 0); issueH(1, 1); issueH(2, 2); issueH(3, 3);
    issueH(4, 4); issueC(4, cslot(4));
    WAIT_V(18); BAR(); issueH(5, 5); issueC(5, cslot(5));
    WAIT_V(20); BAR(); issueH(6, 0); issueC(6, cslot(0));
    WAIT_V(22); BAR(); issueH(7, 1); issueC(7, cslot(1));
    WAIT_V(24); BAR(); issueH(8, 2); issueC(8, cslot(2));
    for (int s = 4; s < 22; s += 6) {
      WAIT_V(24); BAR(); issueH(s + 5, 3); issueC(s + 5, cslot(3));
      WAIT_V(24); BAR(); issueH(s + 6, 4); issueC(s + 6, cslot(4));
      WAIT_V(24); BAR(); issueH(s + 7, 5); issueC(s + 7, cslot(5));
      WAIT_V(24); BAR(); issueH(s + 8, 0); issueC(s + 8, cslot(0));
      WAIT_V(24); BAR(); issueH(s + 9, 1); issueC(s + 9, cslot(1));
      WAIT_V(24); BAR(); issueH(s + 10, 2); issueC(s + 10, cslot(2));
    }
    WAIT_V(24); BAR(); issueH(27, 3); issueC(27, cslot(3));
    WAIT_V(24); BAR(); issueH(28, 4); issueC(28, cslot(4));
    WAIT_V(24); BAR(); issueH(29, 5); issueC(29, cslot(5));
    WAIT_V(24); BAR(); issueH(30, 0); issueC(30, cslot(0));
    WAIT_V(24); BAR(); issueH(31, 1); issueC(31, cslot(1));
    WAIT_V(24); BAR();
    WAIT_V(18); BAR();
    WAIT_V(12); BAR();
    WAIT_V(6); BAR();
    WAIT_V(0); BAR();
    return;
  }
  STAMP(0);
  const int w = wave, fr = lane & 15, fq = lane >> 4;
  const int c0 = fq ^ (fr >> 1);
  const int a0 = fr * 128 + c0 * 16, a1 = fr * 128 + (c0 ^ 4) * 16;
  {
    const int p = 64 * g + 16 * w + fr;
    f32x2 lam, cf, l8, l16, l24, l32, lq, mu_[4];
    {
      float lr = bf2f(Lre[p]), li = bf2f(Lim[p]);
      float dt = bfround(expf(bf2f(logdt[p])));
      float ar = dt * lr, ai = dt * li;
      float e = expf(ar);
      lam = f32x2{e * cosf(ai), e * sinf(ai)};
      float nr = lam.x - 1.0f, ni = lam.y;
      float den = lr * lr + li * li;
      cf = f32x2{(nr * lr + ni * li) / den, (ni * lr - nr * li) / den};
      cd a{(double)lam.x, (double)lam.y};
      a = cmul(a, a); a = cmul(a, a); a = cmul(a, a);
      cd a8 = a, a16 = cmul(a8, a8), a24 = cmul(a16, a8), a32 = cmul(a16, a16), a64 = cmul(a32, a32), a96 = cmul(a64, a32);
      cd mu = cmul(a64, a64);
      l8 = tof(a8); l16 = tof(a16); l24 = tof(a24); l32 = tof(a32);
      lq = fq == 0 ? f32x2{1.f, 0.f} : fq == 1 ? l32 : fq == 2 ? tof(a64) : tof(a96);
      cd mu2 = cmul(mu, mu), mu4 = cmul(mu2, mu2), mu8 = cmul(mu4, mu4);
#pragma unroll
      for (int i = 0; i < 4; ++i) {
        int e2 = c - 1 - fq - 4 * i;
        cd r{1.0, 0.0};
        if (e2 & 1) r = cmul(r, mu);
        if (e2 & 2) r = cmul(r, mu2);
        if (e2 & 4) r = cmul(r, mu4);
        if (e2 & 8) r = cmul(r, mu8);
        mu_[i] = e2 >= 0 ? tof(r) : f32x2{0.f, 0.f};
      }
    }
    asm volatile("" : "+v"(lam), "+v"(cf), "+v"(l8), "+v"(l16), "+v"(l24), "+v"(l32), "+v"(lq));
    asm volatile("" : "+v"(mu_[0]), "+v"(mu_[1]), "+v"(mu_[2]), "+v"(mu_[3]));
    f32x4 acc[8][2];
#pragma unroll
    for (int m = 0; m < 8; ++m) { acc[m][0] = f32x4{0, 0, 0, 0}; acc[m][1] = f32x4{0, 0, 0, 0}; }
    bf16x8 F0[10], F1[10];
    auto rd = [&](bf16x8* F, int base) {
#pragma unroll
      for (int nn = 0; nn < 2; ++nn) F[8 + nn] = *LDSP(bf16x8, base + 16384 + 4096 * w + 2048 * nn);
#pragma unroll
      for (int m = 0; m < 8; ++m) F[m] = *LDSP(bf16x8, base + 2048 * m);
    };
    auto mm = [&](bf16x8* F) {
#pragma unroll
      for (int m = 0; m < 8; ++m)
#pragma unroll
        for (int nn = 0; nn < 2; ++nn) acc[m][nn] = __builtin_amdgcn_mfma_f32_16x16x32_bf16(F[m], F[8 + nn], acc[m][nn], 0, 0, 0);
    };
    STAMP(1);
    BAR();
    STAMP(2);
    rd(F0, a0);
    SB0();
    rd(F1, a1);
    mm(F0);
    ILV(10, 16)
    WAIT_L0();
    for (int s = 1; s < S1; ++s) {
      int buf = (s & 3) * ST1;
      BAR();
      SB0();
      rd(F0, buf + a0);
      mm(F1);
      ILV(10, 16)
      rd(F1, buf + a1);
      mm(F0);
      ILV(10, 16)
      WAIT_L0();
    }
    mm(F1);
    MFMA_DRAIN();
    STAMP(3);
    f32x2 u[32];
#pragma unroll
    for (int m = 0; m < 8; ++m)
#pragma unroll
      for (int j = 0; j < 4; ++j) {
        float a = acc[m][0][j], bb = acc[m][1][j];
        f32x2 t = f32x2{-cf.y, cf.x} * f32x2{bb, bb};
        u[4 * m + j] = __builtin_elementwise_fma(cf, f32x2{a, a}, t);
      }
    f32x2 ag[4];
#pragma unroll
    for (int k = 0; k < 4; ++k) ag[k] = u[8 * k];
#pragma unroll
    for (int i = 1; i < 8; ++i)
#pragma unroll
      for (int k = 0; k < 4; ++k) ag[k] = cfma(lam, ag[k], u[8 * k + i]);
    f32x2 e1 = ag[0], e2 = cfma(l8, e1, ag[1]), e3 = cfma(l8, e2, ag[2]), la = cfma(l8, e3, ag[3]);
    f32x2 q[4];
#pragma unroll
    for (int k = 0; k < 4; ++k) q[k] = f32x2{__shfl(la.x, fr + 16 * k), __shfl(la.y, fr + 16 * k)};
    f32x2 E1 = q[0], E2 = cfma(l32, E1, q[1]), E3 = cfma(l32, E2, q[2]), AG = cfma(l32, E3, q[3]);
    const __amdgpu_buffer_rsrc_t er = __builtin_amdgcn_make_buffer_rsrc((void*)exch, 0, 16 * P * 16, 0x00020000);
    if (fq == 0 && c < 15)
      __builtin_amdgcn_raw_buffer_store_b128(u32x4{__float_as_uint(AG.x), tag.x, __float_as_uint(AG.y), tag.y}, er, (c * P + p) * 16, 0, 16);
    f32x2 E = fq == 0 ? f32x2{0.f, 0.f} : fq == 1 ? E1 : fq == 2 ? E2 : E3;
    f32x2 car = {0.f, 0.f};
    STAMP(4);
    if (c > 0) {
      u32x4 v[4];
      for (unsigned spin = 0;; ++spin) {
        bool ok = true;
#pragma unroll
        for (int i = 0; i < 4; ++i) {
          int j = min(fq + 4 * i, c - 1);
          v[i] = __builtin_amdgcn_raw_buffer_load_b128(er, (j * P + p) * 16, 0, 16);
          ok &= (v[i][1] == tag.x) & (v[i][3] == tag.y);
        }
        if (__all(ok) || spin > (1u << 22)) break;
        __builtin_amdgcn_s_sleep(1);
        asm volatile("" ::: "memory");
      }
#pragma unroll
      for (int i = 0; i < 4; ++i) {
        f32x2 vv = {__uint_as_float(v[i][0]), __uint_as_float(v[i][2])};
        if (fq + 4 * i < c) car = cfma(mu_[i], vv, car);
      }
      car.x += __shfl_xor(car.x, 16); car.y += __shfl_xor(car.y, 16);
      car.x += __shfl_xor(car.x, 32); car.y += __shfl_xor(car.y, 32);
    }
    STAMP(5);
    f32x2 H = cfma(lq, car, E);
    f32x2 hs[4] = {H, cfma(l8, H, e1), cfma(l16, H, e2), cfma(l24, H, e3)};
#pragma unroll
    for (int i = 0; i < 8; ++i)
#pragma unroll
      for (int k = 0; k < 4; ++k) { hs[k] = cfma(lam, hs[k], u[8 * k + i]); u[8 * k + i] = hs[k]; }
#pragma unroll
    for (int i = 0; i < 4; ++i) {
      u32x4 vr, vi;
#pragma unroll
      for (int j = 0; j < 4; ++j) {
        vr[j] = pack2(u[8 * i + 2 * j].x, u[8 * i + 2 * j + 1].x);
        vi[j] = pack2(-u[8 * i + 2 * j].y, -u[8 * i + 2 * j + 1].y);
      }
      *LDSP(u32x4, (16 * w + fr) * IMG_PITCH + 64 * fq + 16 * i) = vr;
      *LDSP(u32x4, (64 + 16 * w + fr) * IMG_PITCH + 64 * fq + 16 * i) = vi;
    }
    WAIT_L0();
    BAR();
    {
      const __amdgpu_buffer_rsrc_t hr = __builtin_amdgcn_make_buffer_rsrc((void*)h, 0, 2048 * 2048 * 2, 0x00020000);
      const int a = (lane >> 2) & 3, bb = lane & 3;
#pragma unroll
      for (int tb = 0; tb < 2; ++tb)
#pragma unroll
        for (int cq = 0; cq < 4; ++cq) {
          int chunk = 4 * cq + fq;
          int tcol = (32 * w + 16 * tb + 4 * bb) * 2;
          int r1 = 8 * chunk + ((fq & 1) ? 4 : 0) + a, r2 = 8 * chunk + ((fq & 1) ? 0 : 4) + a;
          s16x4 v1 = __builtin_amdgcn_ds_read_tr16_b64_v4i16(LDSP(s16x4, r1 * IMG_PITCH + tcol));
          s16x4 v2 = __builtin_amdgcn_ds_read_tr16_b64_v4i16(LDSP(s16x4, r2 * IMG_PITCH + tcol));
          s16x4 lo = (fq & 1) ? v2 : v1, hi = (fq & 1) ? v1 : v2;
          bf16x8 o = __builtin_shufflevector(lo, hi, 0, 1, 2, 3, 4, 5, 6, 7);
          int t = 128 * c + 32 * w + 16 * tb + fr;
          __builtin_amdgcn_raw_buffer_store_b128(__builtin_bit_cast(u32x4, o), hr, (t * 2048 + 128 * g + 8 * chunk) * 2, 0, 16);
        }
    }
    STAMP(6);
    WAIT_V(0);
    if (lane == 0) __hip_atomic_store(&flags[c * 64 + g * 4 + w], tag64, __ATOMIC_RELAXED, __HIP_MEMORY_SCOPE_AGENT);
    STAMP(7);
    BAR();
    BAR();
  }
  const int wd = wave & 1, wt = wave >> 1;
  u32x2 xs[2][4], ds[2];
#pragma unroll
  for (int dm = 0; dm < 2; ++dm) {
    int d = 64 * n + 32 * wd + 16 * dm + 4 * fq;
    ds[dm] = *(const u32x2*)(Dskip + d);
#pragma unroll
    for (int tn = 0; tn < 4; ++tn) xs[dm][tn] = *(const u32x2*)(x + (size_t)(128 * c + 64 * wt + 16 * tn + fr) * D + d);
  }
  f32x4 acc[2][4];
#pragma unroll
  for (int i = 0; i < 2; ++i)
#pragma unroll
    for (int j = 0; j < 4; ++j) acc[i][j] = f32x4{0, 0, 0, 0};
  bf16x8 F0[6], F1[6];
  auto rd = [&](bf16x8* F, int hbase, int cbase) {
#pragma unroll
    for (int i = 0; i < 2; ++i) F[i] = *LDSP(bf16x8, cbase + (32 * wd + 16 * i) * 128);
#pragma unroll
    for (int j = 0; j < 4; ++j) F[2 + j] = *LDSP(bf16x8, hbase + (64 * wt + 16 * j) * 128);
  };
  auto mm = [&](bf16x8* F) {
#pragma unroll
    for (int i = 0; i < 2; ++i)
#pragma unroll
      for (int j = 0; j < 4; ++j) acc[i][j] = __builtin_amdgcn_mfma_f32_16x16x32_bf16(F[i], F[2 + j], acc[i][j], 0, 0, 0);
  };
  BAR();
  STAMP(8);
  rd(F0, a0, cslot(0) + a0);
  SB0();
  rd(F1, a1, cslot(0) + a1);
  mm(F0);
  ILV(6, 8)
  WAIT_L0();
  int bi = 1;
  for (int s = 1; s < S2; ++s) {
    int hb = bi * 16384, cb = bi < 4 ? 131072 + bi * 8192 : 114688 + (bi - 4) * 8192;
    BAR();
    SB0();
    rd(F0, hb + a0, cb + a0);
    mm(F1);
    ILV(6, 8)
    rd(F1, hb + a1, cb + a1);
    mm(F0);
    ILV(6, 8)
    WAIT_L0();
    bi = bi == NS2 - 1 ? 0 : bi + 1;
  }
  mm(F1);
  MFMA_DRAIN();
  STAMP(9);
#pragma unroll
  for (int dm = 0; dm < 2; ++dm) {
    int d = 64 * n + 32 * wd + 16 * dm + 4 * fq;
    float dk[4] = {__uint_as_float(ds[dm][0] << 16), __uint_as_float(ds[dm][0] & 0xffff0000u),
                   __uint_as_float(ds[dm][1] << 16), __uint_as_float(ds[dm][1] & 0xffff0000u)};
#pragma unroll
    for (int tn = 0; tn < 4; ++tn) {
      float xv[4] = {__uint_as_float(xs[dm][tn][0] << 16), __uint_as_float(xs[dm][tn][0] & 0xffff0000u),
                     __uint_as_float(xs[dm][tn][1] << 16), __uint_as_float(xs[dm][tn][1] & 0xffff0000u)};
      f32x4 o;
#pragma unroll
      for (int j = 0; j < 4; ++j) o[j] = 2.0f * acc[dm][tn][j] + bfround(xv[j] * dk[j]);
      *(f32x4*)(y + (size_t)(128 * c + 64 * wt + 16 * tn + fr) * D + d) = o;
    }
  }
}

extern "C" void kernel_launch(void* const* d_in, const int* in_sizes, int n_in, void* d_out, int out_size,
                              void* d_ws, size_t ws_size, hipStream_t stream) {
  const u16* x = (const u16*)d_in[0];
  const u16* Lre = (const u16*)d_in[1];
  const u16* Lim = (const u16*)d_in[2];
  const u16* Bre = (const u16*)d_in[3];
  const u16* Bim = (const u16*)d_in[4];
  const u16* Cre = (const u16*)d_in[5];
  const u16* Cim = (const u16*)d_in[6];
  const u16* logdt = (const u16*)d_in[7];
  const u16* Dskip = (const u16*)d_in[8];
  float* out = (float*)d_out;
  char* ws = (char*)d_ws;
  u32x4* exch = (u32x4*)ws;
  u64* flags = (u64*)(ws + 262144);
  u16* h = (u16*)(ws + (1 << 20));
  (void)hipFuncSetAttribute((const void*)s5_fused, hipFuncAttributeMaxDynamicSharedMemorySize, LDS_BYTES);
  s5_fused<<<256, 512, LDS_BYTES, stream>>>(x, Bre, Bim, Cre, Cim, Lre, Lim, logdt, Dskip, h, exch, flags, out TSPASS);
}
```

```cpp
#include <hip/hip_runtime.h>
#include <hip/hip_bf16.h>
#include <stdint.h>

typedef __attribute__((ext_vector_type(8))) short bf16x8;
typedef __attribute__((ext_vector_type(4))) short s16x4;
typedef __attribute__((ext_vector_type(4))) float f32x4;
typedef __attribute__((ext_vector_type(2))) float f32x2;
typedef __attribute__((ext_vector_type(4))) unsigned u32x4;
typedef __attribute__((ext_vector_type(2))) unsigned u32x2;
typedef unsigned short u16;
typedef unsigned long long u64;
#define LDSP(T, off) ((__attribute__((address_space(3))) T*)(smem + (off)))

#define STAMP(i)
#define LSTAMP(i)
#define TSARG
#define TSPASS

static constexpr int D = 1024, P = 1024;

__device__ __forceinline__ float bf2f(u16 v) { return __uint_as_float(((unsigned)v) << 16); }
__device__ __forceinline__ float bfround(float f) { return __bfloat162float(__float2bfloat16(f)); }
typedef __attribute__((ext_vector_type(2))) __bf16 bf16x2;
__device__ __forceinline__ unsigned pack2(float a, float b) {
  return __builtin_bit_cast(unsigned, __builtin_convertvector(f32x2{a, b}, bf16x2));
}
#define WAIT_V(n) asm volatile("s_waitcnt vmcnt(%0)" ::"i"(n) : "memory")
#define WAIT_L0() asm volatile("s_waitcnt lgkmcnt(0)" ::: "memory")
#define BAR() asm volatile("s_barrier" ::: "memory")
#define SB0() __builtin_amdgcn_sched_barrier(0)
#define MFMA_DRAIN() do { SB0(); asm volatile("s_nop 15\n\ts_nop 15"); SB0(); } while (0)
#define ILV(NR, NM)                                                        \
  _Pragma("unroll") for (int i_ = 0; i_ < NR; ++i_) {                       \
    __builtin_amdgcn_sched_group_barrier(0x8, 1, 0);                        \
    __builtin_amdgcn_sched_group_barrier(0x100, 1, 0);                      \
  }                                                                         \
  __builtin_amdgcn_sched_group_barrier(0x8, NM - NR, 0);                    \
  SB0();
#define GLDS(gp, lo) __builtin_amdgcn_global_load_lds((const unsigned*)(gp), LDSP(unsigned, (lo)), 16, 0, 0)
#define GLDS_SC1(gp, lo) __builtin_amdgcn_global_load_lds((const unsigned*)(gp), LDSP(unsigned, (lo)), 16, 0, 16)

extern "C" __device__ unsigned long long s5_dispatch_id() __asm("llvm.amdgcn.dispatch.id");
__device__ __forceinline__ u32x2 launch_tag() {
  u64 z = s5_dispatch_id() * 0x9E3779B97F4A7C15ull + ((u64)__builtin_amdgcn_dispatch_ptr() >> 6) * 0xD1B54A32D192ED03ull + 0x2545F4914F6CDD1Dull;
  z = (z ^ (z >> 30)) * 0xBF58476D1CE4E5B9ull;
  z = (z ^ (z >> 27)) * 0x94D049BB133111EBull;
  z ^= z >> 31;
  unsigned a = (unsigned)z, b = (unsigned)(z >> 32);
  a = (a & 0xFFFF00FFu) | ((~a & 0xFFu) << 8);
  b = (b & 0xFFFF00FFu) | ((~b & 0xFFu) << 8);
  return u32x2{a, b};
}

struct cd { double r, i; };
__device__ __forceinline__ cd cmul(cd a, cd b) { return cd{a.r * b.r - a.i * b.i, a.r * b.i + a.i * b.r}; }
__device__ __forceinline__ f32x2 tof(cd a) { return f32x2{(float)a.r, (float)a.i}; }
__device__ __forceinline__ f32x2 cfma(f32x2 a, f32x2 b, f32x2 c) {
  f32x2 t = __builtin_elementwise_fma(f32x2{a.x, a.x}, b, c);
  return __builtin_elementwise_fma(f32x2{-a.y, a.y}, f32x2{b.y, b.x}, t);
}

static constexpr int S1 = 16, ST1 = 32768;
static constexpr int S2 = 32, NS2 = 6;
static constexpr int LDS_BYTES = 163840;
static constexpr int IMG_PITCH = 288;
__device__ __forceinline__ constexpr int cslot(int i) { return i < 4 ? 131072 + i * 8192 : 114688 + (i - 4) * 8192; }

__global__ __launch_bounds__(512, 2) void s5_fused(const u16* __restrict__ x, const u16* __restrict__ Bre,
                                                   const u16* __restrict__ Bim, const u16* __restrict__ Cre,
                                                   const u16* __restrict__ Cim, const u16* __restrict__ Lre,
                                                   const u16* __restrict__ Lim, const u16* __restrict__ logdt,
                                                   const u16* __restrict__ Dskip, u16* h, u32x4* exch,
                                                   u64* flags, float* __restrict__ y TSARG) {
  extern __shared__ __attribute__((aligned(16))) char smem[];
  const int b = blockIdx.x, xcd = b & 7, idx = b >> 3;
  const int c = 4 * (xcd & 3) + (idx & 3), g = 8 * (xcd >> 2) + (idx >> 2);
  const int n = g;
  const int wave = __builtin_amdgcn_readfirstlane(threadIdx.x >> 6), lane = threadIdx.x & 63;
  const u32x2 tag = launch_tag();
  const u64 tag64 = (u64)tag.x | ((u64)tag.y << 32);
  if (wave >= 4) {
    const int lw = wave - 4;
    const int koff2 = 0;
    int offC[2];
#pragma unroll
    for (int i = 0; i < 2; ++i) {
      int rho = 16 * lw + 8 * i + (lane >> 3);
      int chunk = (lane & 7) ^ ((rho >> 1) & 7);
      offC[i] = (64 * n + rho) * P + chunk * 8;
    }
    auto issueC = [&](int s0, int slot_off) {
      int s = (s0 + koff2) & 31;
      const u16* Cb = ((s & 1) ? Cim : Cre) + (s >> 1) * 64;
#pragma unroll
      for (int i = 0; i < 2; ++i) GLDS(Cb + offC[i], slot_off + (2 * lw + i) * 1024);
    };
    {
      const u16* src[8];
#pragma unroll
      for (int i = 0; i < 4; ++i) {
        int rho = 32 * lw + 8 * i + (lane >> 3);
        int chunk = (lane & 7) ^ ((rho >> 1) & 7);
        int m = rho >> 4, i16 = rho & 15;
        int tau = 32 * (i16 >> 2) + 4 * m + (i16 & 3);
        src[i] = x + (size_t)(128 * c + tau) * D + chunk * 8;
        int p = 64 * g + 16 * (rho >> 5) + (rho & 15);
        src[4 + i] = ((rho & 16) ? Bim : Bre) + (size_t)p * D + chunk * 8;
      }
      const int koff = 2 * (idx >> 2) + 4 * (idx & 3);
      auto issue = [&](int s0) {
        int buf = (s0 & 3) * ST1;
        int s = (s0 + koff) & 15;
#pragma unroll
        for (int i = 0; i < 4; ++i) GLDS(src[i] + s * 64, buf + (4 * lw + i) * 1024);
#pragma unroll
        for (int i = 0; i < 4; ++i) GLDS(src[4 + i] + s * 64, buf + 16384 + (4 * lw + i) * 1024);
      };
      issue(0); issue(1); issue(2);
      for (int s = 0; s < 6; ++s) { WAIT_V(16); BAR(); issue(s + 3); }
      WAIT_V(16); BAR(); issue(9);
      {
        const char* p1 = (const char*)Cre + b * 8192 + lw * 2048 + lane * 16;
        const char* p2 = (const char*)Cim + b * 8192 + lw * 2048 + lane * 16;
        GLDS(p1, 131072 + lw * 1024); GLDS(p1 + 1024, 131072 + lw * 1024);
        GLDS(p2, 131072 + lw * 1024); GLDS(p2 + 1024, 131072 + lw * 1024);
      }
      WAIT_V(20); BAR(); issue(10);
      WAIT_V(20); BAR(); issue(11);
      WAIT_V(20); BAR(); issue(12);
      WAIT_V(16); BAR(); issue(13);
      WAIT_V(16); BAR(); issue(14);
      WAIT_V(16); BAR(); issue(15);
      issueC(0, cslot(0)); issueC(1, cslot(1)); issueC(2, cslot(2)); issueC(3, cslot(3));
      WAIT_V(24); BAR();
      WAIT_V(16); BAR();
      WAIT_V(8); BAR();
    }
    const u16* srcH[4];
#pragma unroll
    for (int i = 0; i < 4; ++i) {
      int rho = 32 * lw + 8 * i + (lane >> 3);
      int chunk = (lane & 7) ^ ((rho >> 1) & 7);
      srcH[i] = h + (size_t)(128 * c + rho) * 2048 + chunk * 8;
    }
    auto issueH = [&](int s0, int bi) {
      int s = (s0 + koff2) & 31;
#pragma unroll
      for (int i = 0; i < 4; ++i) GLDS_SC1(srcH[i] + s * 64, bi * 16384 + (4 * lw + i) * 1024);
    };
    BAR();
    BAR();
    if (lw == 0) {
      for (unsigned spin = 0;; ++spin) {
        u64 v = __hip_atomic_load(&flags[c * 64 + lane], __ATOMIC_RELAXED, __HIP_MEMORY_SCOPE_AGENT);
        if (__all(v == tag64) || spin > (1u << 22)) break;
        __builtin_amdgcn_s_sleep(4);
      }
    }
    LSTAMP(12);
    BAR();
    issueH(0, 0); issueH(1, 1); issueH(2, 2); issueH(3, 3);
    issueH(4, 4); issueC(4, cslot(4));
    WAIT_V(18); BAR(); issueH(5, 5); issueC(5, cslot(5));
    WAIT_V(20); BAR(); issueH(6, 0); issueC(6, cslot(0));
    WAIT_V(22); BAR(); issueH(7, 1); issueC(7, cslot(1));
    WAIT_V(24); BAR(); issueH(8, 2); issueC(8, cslot(2));
    for (int s = 4; s < 22; s += 6) {
      WAIT_V(24); BAR(); issueH(s + 5, 3); issueC(s + 5, cslot(3));
      WAIT_V(24); BAR(); issueH(s + 6, 4); issueC(s + 6, cslot(4));
      WAIT_V(24); BAR(); issueH(s + 7, 5); issueC(s + 7, cslot(5));
      WAIT_V(24); BAR(); issueH(s + 8, 0); issueC(s + 8, cslot(0));
      WAIT_V(24); BAR(); issueH(s + 9, 1); issueC(s + 9, cslot(1));
      WAIT_V(24); BAR(); issueH(s + 10, 2); issueC(s + 10, cslot(2));
    }
    WAIT_V(24); BAR(); issueH(27, 3); issueC(27, cslot(3));
    WAIT_V(24); BAR(); issueH(28, 4); issueC(28, cslot(4));
    WAIT_V(24); BAR(); issueH(29, 5); issueC(29, cslot(5));
    WAIT_V(24); BAR(); issueH(30, 0); issueC(30, cslot(0));
    WAIT_V(24); BAR(); issueH(31, 1); issueC(31, cslot(1));
    WAIT_V(24); BAR();
    WAIT_V(18); BAR();
    WAIT_V(12); BAR();
    WAIT_V(6); BAR();
    WAIT_V(0); BAR();
    return;
  }
  STAMP(0);
  const int w = wave, fr = lane & 15, fq = lane >> 4;
  const int c0 = fq ^ (fr >> 1);
  const int a0 = fr * 128 + c0 * 16, a1 = fr * 128 + (c0 ^ 4) * 16;
  {
    const int p = 64 * g + 16 * w + fr;
    f32x2 lam, cf, l8, l16, l24, l32, lq, mu4, mue;
    {
      float lr = bf2f(Lre[p]), li = bf2f(Lim[p]);
      float dt = bfround(expf(bf2f(logdt[p])));
      float ar = dt * lr, ai = dt * li;
      float e = expf(ar), sn, cs;
      sincosf(ai, &sn, &cs);
      lam = f32x2{e * cs, e * sn};
      float nr = lam.x - 1.0f, ni = lam.y;
      float den = lr * lr + li * li;
      cf = f32x2{(nr * lr + ni * li) / den, (ni * lr - nr * li) / den};
      lam.y = -lam.y;
      cd a{(double)lam.x, (double)lam.y};
      a = cmul(a, a); a = cmul(a, a); a = cmul(a, a);
      cd a8 = a, a16 = cmul(a8, a8), a24 = cmul(a16, a8), a32 = cmul(a16, a16), a64 = cmul(a32, a32), a96 = cmul(a64, a32);
      cd mu = cmul(a64, a64);
      l8 = tof(a8); l16 = tof(a16); l24 = tof(a24); l32 = tof(a32);
      lq = fq == 0 ? f32x2{1.f, 0.f} : fq == 1 ? l32 : fq == 2 ? tof(a64) : tof(a96);
      cd mu2 = cmul(mu, mu), mu3 = cmul(mu2, mu);
      mu4 = tof(cmul(mu2, mu2));
      int e2 = (c - 1 - fq) & 3;
      mue = e2 == 0 ? f32x2{1.f, 0.f} : e2 == 1 ? tof(mu) : e2 == 2 ? tof(mu2) : tof(mu3);
    }
    asm volatile("" : "+v"(lam), "+v"(cf), "+v"(l8), "+v"(l16), "+v"(l24), "+v"(l32), "+v"(lq), "+v"(mu4), "+v"(mue));
    f32x4 acc[8][2];
#pragma unroll
    for (int m = 0; m < 8; ++m) { acc[m][0] = f32x4{0, 0, 0, 0}; acc[m][1] = f32x4{0, 0, 0, 0}; }
    bf16x8 F0[10], F1[10];
    auto rd = [&](bf16x8* F, int base) {
#pragma unroll
      for (int nn = 0; nn < 2; ++nn) F[8 + nn] = *LDSP(bf16x8, base + 16384 + 4096 * w + 2048 * nn);
#pragma unroll
      for (int m = 0; m < 8; ++m) F[m] = *LDSP(bf16x8, base + 2048 * m);
    };
    auto mm = [&](bf16x8* F) {
#pragma unroll
      for (int m = 0; m < 8; ++m)
#pragma unroll
        for (int nn = 0; nn < 2; ++nn) acc[m][nn] = __builtin_amdgcn_mfma_f32_16x16x32_bf16(F[m], F[8 + nn], acc[m][nn], 0, 0, 0);
    };
    STAMP(1);
    BAR();
    STAMP(2);
    rd(F0, a0);
    SB0();
    rd(F1, a1);
    mm(F0);
    ILV(10, 16)
    WAIT_L0();
    for (int s = 1; s < S1; ++s) {
      int buf = (s & 3) * ST1;
      BAR();
      SB0();
      rd(F0, buf + a0);
      mm(F1);
      ILV(10, 16)
      rd(F1, buf + a1);
      mm(F0);
      ILV(10, 16)
      WAIT_L0();
    }
    mm(F1);
    MFMA_DRAIN();
    STAMP(3);
    f32x2 u[32];
#pragma unroll
    for (int m = 0; m < 8; ++m)
#pragma unroll
      for (int j = 0; j < 4; ++j) {
        float a = acc[m][0][j], bb = acc[m][1][j];
        f32x2 t = f32x2{-cf.y, -cf.x} * f32x2{bb, bb};
        u[4 * m + j] = __builtin_elementwise_fma(f32x2{cf.x, -cf.y}, f32x2{a, a}, t);
      }
    f32x2 ag[4];
#pragma unroll
    for (int k = 0; k < 4; ++k) ag[k] = u[8 * k];
#pragma unroll
    for (int i = 1; i < 8; ++i)
#pragma unroll
      for (int k = 0; k < 4; ++k) ag[k] = cfma(lam, ag[k], u[8 * k + i]);
    f32x2 e1 = ag[0], e2 = cfma(l8, e1, ag[1]), e3 = cfma(l8, e2, ag[2]), la = cfma(l8, e3, ag[3]);
    f32x2 q[4];
#pragma unroll
    for (int k = 0; k < 4; ++k) q[k] = f32x2{__shfl(la.x, fr + 16 * k), __shfl(la.y, fr + 16 * k)};
    f32x2 E1 = q[0], E2 = cfma(l32, E1, q[1]), E3 = cfma(l32, E2, q[2]), AG = cfma(l32, E3, q[3]);
    const __amdgpu_buffer_rsrc_t er = __builtin_amdgcn_make_buffer_rsrc((void*)exch, 0, 16 * P * 16, 0x00020000);
    if (fq == 0 && c < 15)
      __builtin_amdgcn_raw_buffer_store_b128(u32x4{__float_as_uint(AG.x), tag.x, __float_as_uint(AG.y), tag.y}, er, (c * P + p) * 16, 0, 16);
    f32x2 E = fq == 0 ? f32x2{0.f, 0.f} : fq == 1 ? E1 : fq == 2 ? E2 : E3;
    f32x2 car = {0.f, 0.f};
    STAMP(4);
    if (c > 0) {
      u32x4 v[4];
      for (unsigned spin = 0;; ++spin) {
        bool ok = true;
#pragma unroll
        for (int i = 0; i < 4; ++i) {
          int j = min(fq + 4 * i, c - 1);
          v[i] = __builtin_amdgcn_raw_buffer_load_b128(er, (j * P + p) * 16, 0, 16);
          ok &= (v[i][1] == tag.x) & (v[i][3] == tag.y);
        }
        if (__all(ok) || spin > (1u << 22)) break;
        __builtin_amdgcn_s_sleep(1);
        asm volatile("" ::: "memory");
      }
#pragma unroll
      for (int i = 0; i < 4; ++i) {
        f32x2 vv = {__uint_as_float(v[i][0]), __uint_as_float(v[i][2])};
        if (fq + 4 * i < c) car = cfma(mu4, car, vv);
      }
      car = cfma(mue, car, f32x2{0.f, 0.f});
      car.x += __shfl_xor(car.x, 16); car.y += __shfl_xor(car.y, 16);
      car.x += __shfl_xor(car.x, 32); car.y += __shfl_xor(car.y, 32);
    }
    STAMP(5);
    f32x2 H = cfma(lq, car, E);
    f32x2 hs[4] = {H, cfma(l8, H, e1), cfma(l16, H, e2), cfma(l24, H, e3)};
#pragma unroll
    for (int i = 0; i < 8; ++i)
#pragma unroll
      for (int k = 0; k < 4; ++k) { hs[k] = cfma(lam, hs[k], u[8 * k + i]); u[8 * k + i] = hs[k]; }
#pragma unroll
    for (int i = 0; i < 4; ++i) {
      u32x4 vr, vi;
#pragma unroll
      for (int j = 0; j < 4; ++j) {
        vr[j] = pack2(u[8 * i + 2 * j].x, u[8 * i + 2 * j + 1].x);
        vi[j] = pack2(u[8 * i + 2 * j].y, u[8 * i + 2 * j + 1].y);
      }
      *LDSP(u32x4, (16 * w + fr) * IMG_PITCH + 64 * fq + 16 * i) = vr;
      *LDSP(u32x4, (64 + 16 * w + fr) * IMG_PITCH + 64 * fq + 16 * i) = vi;
    }
    WAIT_L0();
    BAR();
    {
      const __amdgpu_buffer_rsrc_t hr = __builtin_amdgcn_make_buffer_rsrc((void*)h, 0, 2048 * 2048 * 2, 0x00020000);
      const int a = (lane >> 2) & 3, bb = lane & 3;
      const int ib = (8 * fq + a) * IMG_PITCH + (32 * w + 4 * bb) * 2;
      bf16x8 o[8];
#pragma unroll
      for (int tb = 0; tb < 2; ++tb)
#pragma unroll
        for (int cq = 0; cq < 4; ++cq) {
          s16x4 lo = __builtin_amdgcn_ds_read_tr16_b64_v4i16(LDSP(s16x4, ib + (32 * cq) * IMG_PITCH + 32 * tb));
          s16x4 hi = __builtin_amdgcn_ds_read_tr16_b64_v4i16(LDSP(s16x4, ib + (32 * cq + 4) * IMG_PITCH + 32 * tb));
          o[4 * tb + cq] = __builtin_shufflevector(lo, hi, 0, 1, 2, 3, 4, 5, 6, 7);
        }
      SB0();
      const int ob = ((128 * c + 32 * w + fr) * 2048 + 128 * g + 8 * fq) * 2;
#pragma unroll
      for (int tb = 0; tb < 2; ++tb)
#pragma unroll
        for (int cq = 0; cq < 4; ++cq)
          __builtin_amdgcn_raw_buffer_store_b128(__builtin_bit_cast(u32x4, o[4 * tb + cq]), hr, ob + tb * 65536 + cq * 64, 0, 16);
    }
    STAMP(6);
    WAIT_V(0);
    if (lane == 0) __hip_atomic_store(&flags[c * 64 + g * 4 + w], tag64, __ATOMIC_RELAXED, __HIP_MEMORY_SCOPE_AGENT);
    STAMP(7);
    BAR();
    BAR();
  }
  const int wd = wave & 1, wt = wave >> 1;
  u32x2 xs[2][4], ds[2];
#pragma unroll
  for (int dm = 0; dm < 2; ++dm) {
    int d = 64 * n + 32 * wd + 16 * dm + 4 * fq;
    ds[dm] = *(const u32x2*)(Dskip + d);
#pragma unroll
    for (int tn = 0; tn < 4; ++tn) xs[dm][tn] = *(const u32x2*)(x + (size_t)(128 * c + 64 * wt + 16 * tn + fr) * D + d);
  }
  f32x4 acc[2][4];
#pragma unroll
  for (int i = 0; i < 2; ++i)
#pragma unroll
    for (int j = 0; j < 4; ++j) acc[i][j] = f32x4{0, 0, 0, 0};
  bf16x8 F0[6], F1[6];
  auto rd = [&](bf16x8* F, int hbase, int cbase) {
#pragma unroll
    for (int i = 0; i < 2; ++i) F[i] = *LDSP(bf16x8, cbase + (32 * wd + 16 * i) * 128);
#pragma unroll
    for (int j = 0; j < 4; ++j) F[2 + j] = *LDSP(bf16x8, hbase + (64 * wt + 16 * j) * 128);
  };
  auto mm = [&](bf16x8* F) {
#pragma unroll
    for (int i = 0; i < 2; ++i)
#pragma unroll
      for (int j = 0; j < 4; ++j) acc[i][j] = __builtin_amdgcn_mfma_f32_16x16x32_bf16(F[i], F[2 + j], acc[i][j], 0, 0, 0);
  };
  BAR();
  STAMP(8);
  rd(F0, a0, cslot(0) + a0);
  SB0();
  rd(F1, a1, cslot(0) + a1);
  mm(F0);
  ILV(6, 8)
  WAIT_L0();
  int bi = 1;
  for (int s = 1; s < S2; ++s) {
    int hb = bi * 16384, cb = bi < 4 ? 131072 + bi * 8192 : 114688 + (bi - 4) * 8192;
    BAR();
    SB0();
    rd(F0, hb + a0, cb + a0);
    mm(F1);
    ILV(6, 8)
    rd(F1, hb + a1, cb + a1);
    mm(F0);
    ILV(6, 8)
    WAIT_L0();
    bi = bi == NS2 - 1 ? 0 : bi + 1;
  }
  mm(F1);
  MFMA_DRAIN();
  STAMP(9);
  const __amdgpu_buffer_rsrc_t yr = __builtin_amdgcn_make_buffer_rsrc((void*)y, 0, 2048 * 1024 * 4, 0x00020000);
#pragma unroll
  for (int dm = 0; dm < 2; ++dm) {
    int d = 64 * n + 32 * wd + 16 * dm + 4 * fq;
    float dk[4] = {__uint_as_float(ds[dm][0] << 16), __uint_as_float(ds[dm][0] & 0xffff0000u),
                   __uint_as_float(ds[dm][1] << 16), __uint_as_float(ds[dm][1] & 0xffff0000u)};
#pragma unroll
    for (int tn = 0; tn < 4; ++tn) {
      float xv[4] = {__uint_as_float(xs[dm][tn][0] << 16), __uint_as_float(xs[dm][tn][0] & 0xffff0000u),
                     __uint_as_float(xs[dm][tn][1] << 16), __uint_as_float(xs[dm][tn][1] & 0xffff0000u)};
      f32x4 o;
#pragma unroll
      for (int j = 0; j < 4; ++j) o[j] = 2.0f * acc[dm][tn][j] + bfround(xv[j] * dk[j]);
      __builtin_amdgcn_raw_buffer_store_b128(__builtin_bit_cast(u32x4, o), yr, ((128 * c + 64 * wt + 16 * tn + fr) * D + d) * 4, 0, 16);
    }
  }
}

extern "C" void kernel_launch(void* const* d_in, const int* in_sizes, int n_in, void* d_out, int out_size,
                              void* d_ws, size_t ws_size, hipStream_t stream) {
  const u16* x = (const u16*)d_in[0];
  const u16* Lre = (const u16*)d_in[1];
  const u16* Lim = (const u16*)d_in[2];
  const u16* Bre = (const u16*)d_in[3];
  const u16* Bim = (const u16*)d_in[4];
  const u16* Cre = (const u16*)d_in[5];
  const u16* Cim = (const u16*)d_in[6];
  const u16* logdt = (const u16*)d_in[7];
  const u16* Dskip = (const u16*)d_in[8];
  float* out = (float*)d_out;
  char* ws = (char*)d_ws;
  u32x4* exch = (u32x4*)ws;
  u64* flags = (u64*)(ws + 262144);
  u16* h = (u16*)(ws + (1 << 20));
  (void)hipFuncSetAttribute((const void*)s5_fused, hipFuncAttributeMaxDynamicSharedMemorySize, LDS_BYTES);
  s5_fused<<<256, 512, LDS_BYTES, stream>>>(x, Bre, Bim, Cre, Cim, Lre, Lim, logdt, Dskip, h, exch, flags, out TSPASS);
}
```

```cpp
#include <hip/hip_runtime.h>
#include <hip/hip_bf16.h>
#include <stdint.h>

typedef __attribute__((ext_vector_type(8))) short bf16x8;
typedef __attribute__((ext_vector_type(4))) short s16x4;
typedef __attribute__((ext_vector_type(4))) float f32x4;
typedef __attribute__((ext_vector_type(2))) float f32x2;
typedef __attribute__((ext_vector_type(4))) unsigned u32x4;
typedef __attribute__((ext_vector_type(2))) unsigned u32x2;
typedef unsigned short u16;
typedef unsigned long long u64;
#define LDSP(T, off) ((__attribute__((address_space(3))) T*)(smem + (off)))

#define STAMP(i)
#define LSTAMP(i)
#define TSARG
#define TSPASS

static constexpr int D = 1024, P = 1024;

__device__ __forceinline__ float bf2f(u16 v) { return __uint_as_float(((unsigned)v) << 16); }
__device__ __forceinline__ float bfround(float f) { return __bfloat162float(__float2bfloat16(f)); }
typedef __attribute__((ext_vector_type(2))) __bf16 bf16x2;
__device__ __forceinline__ unsigned pack2(float a, float b) {
  return __builtin_bit_cast(unsigned, __builtin_convertvector(f32x2{a, b}, bf16x2));
}
#define WAIT_V(n) asm volatile("s_waitcnt vmcnt(%0)" ::"i"(n) : "memory")
#define WAIT_L0() asm volatile("s_waitcnt lgkmcnt(0)" ::: "memory")
#define BAR() asm volatile("s_barrier" ::: "memory")
#define SB0() __builtin_amdgcn_sched_barrier(0)
#define MFMA_DRAIN() do { SB0(); asm volatile("s_nop 15\n\ts_nop 15"); SB0(); } while (0)
#define ILV(NR, NM)                                                        \
  _Pragma("unroll") for (int i_ = 0; i_ < NR; ++i_) {                       \
    __builtin_amdgcn_sched_group_barrier(0x8, 1, 0);                        \
    __builtin_amdgcn_sched_group_barrier(0x100, 1, 0);                      \
  }                                                                         \
  __builtin_amdgcn_sched_group_barrier(0x8, NM - NR, 0);                    \
  SB0();
#define GLDS(gp, lo) __builtin_amdgcn_global_load_lds((const unsigned*)(gp), LDSP(unsigned, (lo)), 16, 0, 0)
#define GLDS_SC1(gp, lo) __builtin_amdgcn_global_load_lds((const unsigned*)(gp), LDSP(unsigned, (lo)), 16, 0, 16)

extern "C" __device__ unsigned long long s5_dispatch_id() __asm("llvm.amdgcn.dispatch.id");
__device__ __forceinline__ u32x2 launch_tag() {
  u64 z = s5_dispatch_id() * 0x9E3779B97F4A7C15ull + ((u64)__builtin_amdgcn_dispatch_ptr() >> 6) * 0xD1B54A32D192ED03ull + 0x2545F4914F6CDD1Dull;
  z = (z ^ (z >> 30)) * 0xBF58476D1CE4E5B9ull;
  z = (z ^ (z >> 27)) * 0x94D049BB133111EBull;
  z ^= z >> 31;
  unsigned a = (unsigned)z, b = (unsigned)(z >> 32);
  a = (a & 0xFFFF00FFu) | ((~a & 0xFFu) << 8);
  b = (b & 0xFFFF00FFu) | ((~b & 0xFFu) << 8);
  return u32x2{a, b};
}

struct cd { double r, i; };
__device__ __forceinline__ cd cmul(cd a, cd b) { return cd{a.r * b.r - a.i * b.i, a.r * b.i + a.i * b.r}; }
__device__ __forceinline__ f32x2 tof(cd a) { return f32x2{(float)a.r, (float)a.i}; }
__device__ __forceinline__ f32x2 cfma(f32x2 a, f32x2 b, f32x2 c) {
  f32x2 t = __builtin_elementwise_fma(f32x2{a.x, a.x}, b, c);
  return __builtin_elementwise_fma(f32x2{-a.y, a.y}, f32x2{b.y, b.x}, t);
}

static constexpr int S1 = 16, ST1 = 32768;
static constexpr int S2 = 32, NS2 = 6;
static constexpr int LDS_BYTES = 163840;
static constexpr int IMG_PITCH = 288;
__device__ __forceinline__ constexpr int cslot(int i) { return i < 4 ? 131072 + i * 8192 : 98304 + (i - 4) * 8192; }

__global__ __launch_bounds__(512, 2) void s5_fused(const u16* __restrict__ x, const u16* __restrict__ Bre,
                                                   const u16* __restrict__ Bim, const u16* __restrict__ Cre,
                                                   const u16* __restrict__ Cim, const u16* __restrict__ Lre,
                                                   const u16* __restrict__ Lim, const u16* __restrict__ logdt,
                                                   const u16* __restrict__ Dskip, u16* h, u32x4* exch,
                                                   u64* flags, float* __restrict__ y TSARG) {
  extern __shared__ __attribute__((aligned(16))) char smem[];
  const int b = blockIdx.x, xcd = b & 7, idx = b >> 3;
  const int c = 4 * (xcd & 3) + (idx & 3), g = 8 * (xcd >> 2) + (idx >> 2);
  const int n = g;
  const int wave = __builtin_amdgcn_readfirstlane(threadIdx.x >> 6), lane = threadIdx.x & 63;
  const u32x2 tag = launch_tag();
  const u64 tag64 = (u64)tag.x | ((u64)tag.y << 32);
  auto coff = [&](int s) { int gb = s < 2 ? g : (((s - 2) >> 1) < g ? ((s - 2) >> 1) : (s >> 1)); return gb * 64; };
  if (wave >= 4) {
    const int lw = wave - 4;
    int offC[2];
#pragma unroll
    for (int i = 0; i < 2; ++i) {
      int rho = 16 * lw + 8 * i + (lane >> 3);
      int chunk = (lane & 7) ^ ((rho >> 1) & 7);
      offC[i] = (64 * n + rho) * P + chunk * 8;
    }
    auto issueC = [&](int s, int slot_off) {
      const u16* Cb = ((s & 1) ? Cim : Cre) + coff(s);
#pragma unroll
      for (int i = 0; i < 2; ++i) GLDS(Cb + offC[i], slot_off + (2 * lw + i) * 1024);
    };
    {
      const u16* src[8];
#pragma unroll
      for (int i = 0; i < 4; ++i) {
        int rho = 32 * lw + 8 * i + (lane >> 3);
        int chunk = (lane & 7) ^ ((rho >> 1) & 7);
        int m = rho >> 4, i16 = rho & 15;
        int tau = 32 * (i16 >> 2) + 4 * m + (i16 & 3);
        src[i] = x + (size_t)(128 * c + tau) * D + chunk * 8;
        int p = 64 * g + 16 * (rho >> 5) + (rho & 15);
        src[4 + i] = ((rho & 16) ? Bim : Bre) + (size_t)p * D + chunk * 8;
      }
      const int koff = 2 * (idx >> 2) + 4 * (idx & 3);
      auto issue = [&](int s0) {
        int buf = (s0 & 3) * ST1;
        int s = (s0 + koff) & 15;
#pragma unroll
        for (int i = 0; i < 4; ++i) GLDS(src[i] + s * 64, buf + (4 * lw + i) * 1024);
#pragma unroll
        for (int i = 0; i < 4; ++i) GLDS(src[4 + i] + s * 64, buf + 16384 + (4 * lw + i) * 1024);
      };
      issue(0); issue(1); issue(2);
      for (int s = 0; s < 6; ++s) { WAIT_V(16); BAR(); issue(s + 3); }
      WAIT_V(16); BAR(); issue(9);
      {
        const char* p1 = (const char*)Cre + b * 8192 + lw * 2048 + lane * 16;
        const char* p2 = (const char*)Cim + b * 8192 + lw * 2048 + lane * 16;
        GLDS(p1, 131072 + lw * 1024); GLDS(p1 + 1024, 131072 + lw * 1024);
        GLDS(p2, 131072 + lw * 1024); GLDS(p2 + 1024, 131072 + lw * 1024);
      }
      WAIT_V(20); BAR(); issue(10);
      WAIT_V(20); BAR(); issue(11);
      WAIT_V(20); BAR(); issue(12);
      WAIT_V(16); BAR(); issue(13);
      WAIT_V(16); BAR(); issue(14);
      WAIT_V(16); BAR(); issue(15);
      issueC(25, cslot(0)); issueC(26, cslot(1)); issueC(27, cslot(2)); issueC(28, cslot(3));
      WAIT_V(24); BAR();
      WAIT_V(16); BAR();
      WAIT_V(0); BAR();
    }
    const u16* srcH[4];
#pragma unroll
    for (int i = 0; i < 4; ++i) {
      int rho = 32 * lw + 8 * i + (lane >> 3);
      int chunk = (lane & 7) ^ ((rho >> 1) & 7);
      srcH[i] = h + (size_t)(128 * c + rho) * 2048 + chunk * 8;
    }
    auto issueH = [&](int s, int bi) {
      const int ho = coff(s) * 2 + (s & 1) * 64;
#pragma unroll
      for (int i = 0; i < 4; ++i) GLDS_SC1(srcH[i] + ho, bi * 16384 + (4 * lw + i) * 1024);
    };
    issueC(29, 40960); issueC(30, 49152); issueC(31, 57344);
    BAR();
    issueC(0, cslot(0)); issueC(1, cslot(1));
    WAIT_V(0);
    BAR();
    if (lw == 0) {
      for (unsigned spin = 0;; ++spin) {
        u64 v = __hip_atomic_load(&flags[(c * 16 + (lane >> 2)) * 16 + (lane & 3)], __ATOMIC_RELAXED, __HIP_MEMORY_SCOPE_AGENT);
        if (__all(v == tag64) || spin > (1u << 22)) break;
        __builtin_amdgcn_s_sleep(2);
      }
    }
#pragma unroll
    for (int k = 2; k < 8; ++k) issueC(k, cslot(k));
    BAR();
    issueH(2, 0);
    issueH(3, 1);
    issueH(4, 2);
    issueH(5, 3);
    WAIT_V(12); BAR();
    issueH(6, 4);
    issueC(8, cslot(0));
    issueH(7, 5);
    issueC(9, cslot(1));
    WAIT_V(20); BAR();
    issueH(8, 0);
    issueC(10, cslot(2));
    WAIT_V(22); BAR();
    issueH(9, 1);
    issueC(11, cslot(3));
    WAIT_V(24); BAR();
    issueH(10, 2);
    issueC(12, cslot(4));
    WAIT_V(26); BAR();
    issueH(11, 3);
    issueC(13, cslot(5));
    WAIT_V(26); BAR();
    issueH(12, 4);
    issueC(14, cslot(6));
    WAIT_V(26); BAR();
    issueH(13, 5);
    issueC(15, cslot(7));
    WAIT_V(26); BAR();
    issueH(14, 0);
    issueC(16, cslot(0));
    WAIT_V(26); BAR();
    issueH(15, 1);
    issueC(17, cslot(1));
    WAIT_V(26); BAR();
    issueH(16, 2);
    issueC(18, cslot(2));
    WAIT_V(26); BAR();
    issueH(17, 3);
    issueC(19, cslot(3));
    WAIT_V(26); BAR();
    issueH(18, 4);
    issueC(20, cslot(4));
    WAIT_V(26); BAR();
    issueH(19, 5);
    issueC(21, cslot(5));
    WAIT_V(26); BAR();
    issueH(20, 0);
    issueC(22, cslot(6));
    WAIT_V(26); BAR();
    issueH(21, 1);
    issueC(23, cslot(7));
    WAIT_V(26); BAR();
    issueH(22, 2);
    issueC(24, cslot(0));
    WAIT_V(26); BAR();
    issueH(23, 3);
    WAIT_V(24); BAR();
    issueH(24, 4);
    WAIT_V(22); BAR();
    issueH(25, 5);
    WAIT_V(20); BAR();
    issueH(26, 0);
    WAIT_V(18); BAR();
    issueH(27, 1);
    WAIT_V(16); BAR();
    issueH(28, 2);
    WAIT_V(16); BAR();
    issueH(29, 3);
    WAIT_V(16); BAR();
    issueH(30, 4);
    WAIT_V(16); BAR();
    issueH(31, 5);
    WAIT_V(16); BAR();
    WAIT_V(12); BAR();
    WAIT_V(8); BAR();
    WAIT_V(4); BAR();
    WAIT_V(0); BAR();
    return;
  }
  STAMP(0);
  const int w = wave, fr = lane & 15, fq = lane >> 4;
  const int c0 = fq ^ (fr >> 1);
  const int a0 = fr * 128 + c0 * 16, a1 = fr * 128 + (c0 ^ 4) * 16;
  bf16x8 CR[7][2][2];
  {
    const int p = 64 * g + 16 * w + fr;
    f32x2 lam, cf, l8, l16, l24, l32, lq, mu4, mue;
    {
      float lr = bf2f(Lre[p]), li = bf2f(Lim[p]);
      float dt = bfround(expf(bf2f(logdt[p])));
      float ar = dt * lr, ai = dt * li;
      float e = expf(ar), sn, cs;
      sincosf(ai, &sn, &cs);
      lam = f32x2{e * cs, e * sn};
      float nr = lam.x - 1.0f, ni = lam.y;
      float den = lr * lr + li * li;
      cf = f32x2{(nr * lr + ni * li) / den, (ni * lr - nr * li) / den};
      lam.y = -lam.y;
      cd a{(double)lam.x, (double)lam.y};
      a = cmul(a, a); a = cmul(a, a); a = cmul(a, a);
      cd a8 = a, a16 = cmul(a8, a8), a24 = cmul(a16, a8), a32 = cmul(a16, a16), a64 = cmul(a32, a32), a96 = cmul(a64, a32);
      cd mu = cmul(a64, a64);
      l8 = tof(a8); l16 = tof(a16); l24 = tof(a24); l32 = tof(a32);
      lq = fq == 0 ? f32x2{1.f, 0.f} : fq == 1 ? l32 : fq == 2 ? tof(a64) : tof(a96);
      cd mu2 = cmul(mu, mu), mu3 = cmul(mu2, mu);
      mu4 = tof(cmul(mu2, mu2));
      int e2 = (c - 1 - fq) & 3;
      mue = e2 == 0 ? f32x2{1.f, 0.f} : e2 == 1 ? tof(mu) : e2 == 2 ? tof(mu2) : tof(mu3);
    }
    asm volatile("" : "+v"(lam), "+v"(cf), "+v"(l8), "+v"(l16), "+v"(l24), "+v"(l32), "+v"(lq), "+v"(mu4), "+v"(mue));
    f32x4 acc[8][2];
#pragma unroll
    for (int m = 0; m < 8; ++m) { acc[m][0] = f32x4{0, 0, 0, 0}; acc[m][1] = f32x4{0, 0, 0, 0}; }
    bf16x8 F0[10], F1[10];
    auto rd = [&](bf16x8* F, int base) {
#pragma unroll
      for (int nn = 0; nn < 2; ++nn) F[8 + nn] = *LDSP(bf16x8, base + 16384 + 4096 * w + 2048 * nn);
#pragma unroll
      for (int m = 0; m < 8; ++m) F[m] = *LDSP(bf16x8, base + 2048 * m);
    };
    auto mm = [&](bf16x8* F) {
#pragma unroll
      for (int m = 0; m < 8; ++m)
#pragma unroll
        for (int nn = 0; nn < 2; ++nn) acc[m][nn] = __builtin_amdgcn_mfma_f32_16x16x32_bf16(F[m], F[8 + nn], acc[m][nn], 0, 0, 0);
    };
    STAMP(1);
    BAR();
    STAMP(2);
    rd(F0, a0);
    SB0();
    rd(F1, a1);
    mm(F0);
    ILV(10, 16)
    WAIT_L0();
    for (int s = 1; s < S1; ++s) {
      int buf = (s & 3) * ST1;
      BAR();
      SB0();
      rd(F0, buf + a0);
      mm(F1);
      ILV(10, 16)
      rd(F1, buf + a1);
      mm(F0);
      ILV(10, 16)
      WAIT_L0();
    }
    mm(F1);
    MFMA_DRAIN();
    STAMP(3);
    f32x2 u[32];
#pragma unroll
    for (int m = 0; m < 8; ++m)
#pragma unroll
      for (int j = 0; j < 4; ++j) {
        float a = acc[m][0][j], bb = acc[m][1][j];
        f32x2 t = f32x2{-cf.y, -cf.x} * f32x2{bb, bb};
        u[4 * m + j] = __builtin_elementwise_fma(f32x2{cf.x, -cf.y}, f32x2{a, a}, t);
      }
    f32x2 ag[4];
#pragma unroll
    for (int k = 0; k < 4; ++k) ag[k] = u[8 * k];
#pragma unroll
    for (int i = 1; i < 8; ++i)
#pragma unroll
      for (int k = 0; k < 4; ++k) ag[k] = cfma(lam, ag[k], u[8 * k + i]);
    f32x2 e1 = ag[0], e2 = cfma(l8, e1, ag[1]), e3 = cfma(l8, e2, ag[2]), la = cfma(l8, e3, ag[3]);
    f32x2 q[4];
#pragma unroll
    for (int k = 0; k < 4; ++k) q[k] = f32x2{__shfl(la.x, fr + 16 * k), __shfl(la.y, fr + 16 * k)};
    f32x2 E1 = q[0], E2 = cfma(l32, E1, q[1]), E3 = cfma(l32, E2, q[2]), AG = cfma(l32, E3, q[3]);
    const __amdgpu_buffer_rsrc_t er = __builtin_amdgcn_make_buffer_rsrc((void*)exch, 0, 16 * P * 16, 0x00020000);
    if (fq == 0 && c < 15)
      __builtin_amdgcn_raw_buffer_store_b128(u32x4{__float_as_uint(AG.x), tag.x, __float_as_uint(AG.y), tag.y}, er, (c * P + p) * 16, 0, 16);
    f32x2 E = fq == 0 ? f32x2{0.f, 0.f} : fq == 1 ? E1 : fq == 2 ? E2 : E3;
    f32x2 car = {0.f, 0.f};
    STAMP(4);
#pragma unroll
    for (int r = 0; r < 4; ++r) {
#pragma unroll
      for (int i = 0; i < 2; ++i) {
        CR[r][0][i] = *LDSP(bf16x8, cslot(r) + a0 + (32 * (w & 1) + 16 * i) * 128);
        CR[r][1][i] = *LDSP(bf16x8, cslot(r) + a1 + (32 * (w & 1) + 16 * i) * 128);
      }
      asm volatile("" : "+v"(CR[r][0][0]), "+v"(CR[r][0][1]), "+v"(CR[r][1][0]), "+v"(CR[r][1][1]));
    }
    if (c > 0) {
      u32x4 v[4];
      for (unsigned spin = 0;; ++spin) {
        bool ok = true;
#pragma unroll
        for (int i = 0; i < 4; ++i) {
          int j = min(fq + 4 * i, c - 1);
          v[i] = __builtin_amdgcn_raw_buffer_load_b128(er, (j * P + p) * 16, 0, 16);
          ok &= (v[i][1] == tag.x) & (v[i][3] == tag.y);
        }
        if (__all(ok) || spin > (1u << 22)) break;
        __builtin_amdgcn_s_sleep(1);
        asm volatile("" ::: "memory");
      }
#pragma unroll
      for (int i = 0; i < 4; ++i) {
        f32x2 vv = {__uint_as_float(v[i][0]), __uint_as_float(v[i][2])};
        if (fq + 4 * i < c) car = cfma(mu4, car, vv);
      }
      car = cfma(mue, car, f32x2{0.f, 0.f});
      car.x += __shfl_xor(car.x, 16); car.y += __shfl_xor(car.y, 16);
      car.x += __shfl_xor(car.x, 32); car.y += __shfl_xor(car.y, 32);
    }
    STAMP(5);
    f32x2 H = cfma(lq, car, E);
    f32x2 hs[4] = {H, cfma(l8, H, e1), cfma(l16, H, e2), cfma(l24, H, e3)};
#pragma unroll
    for (int i = 0; i < 8; ++i)
#pragma unroll
      for (int k = 0; k < 4; ++k) { hs[k] = cfma(lam, hs[k], u[8 * k + i]); u[8 * k + i] = hs[k]; }
#pragma unroll
    for (int i = 0; i < 4; ++i) {
      u32x4 vr, vi;
#pragma unroll
      for (int j = 0; j < 4; ++j) {
        vr[j] = pack2(u[8 * i + 2 * j].x, u[8 * i + 2 * j + 1].x);
        vi[j] = pack2(u[8 * i + 2 * j].y, u[8 * i + 2 * j + 1].y);
      }
      *LDSP(u32x4, (16 * w + fr) * IMG_PITCH + 64 * fq + 16 * i) = vr;
      *LDSP(u32x4, (64 + 16 * w + fr) * IMG_PITCH + 64 * fq + 16 * i) = vi;
    }
    WAIT_L0();
    BAR();
    {
      const __amdgpu_buffer_rsrc_t hr = __builtin_amdgcn_make_buffer_rsrc((void*)h, 0, 2048 * 2048 * 2, 0x00020000);
      const int a = (lane >> 2) & 3, bb = lane & 3;
      const int ib = (8 * fq + a) * IMG_PITCH + (32 * w + 4 * bb) * 2;
      bf16x8 o[8];
#pragma unroll
      for (int tb = 0; tb < 2; ++tb)
#pragma unroll
        for (int cq = 0; cq < 4; ++cq) {
          s16x4 lo = __builtin_amdgcn_ds_read_tr16_b64_v4i16(LDSP(s16x4, ib + (32 * cq) * IMG_PITCH + 32 * tb));
          s16x4 hi = __builtin_amdgcn_ds_read_tr16_b64_v4i16(LDSP(s16x4, ib + (32 * cq + 4) * IMG_PITCH + 32 * tb));
          o[4 * tb + cq] = __builtin_shufflevector(lo, hi, 0, 1, 2, 3, 4, 5, 6, 7);
        }
      SB0();
      const int ob = ((128 * c + 32 * w + fr) * 2048 + 128 * g + 8 * fq) * 2;
#pragma unroll
      for (int tb = 0; tb < 2; ++tb)
#pragma unroll
        for (int cq = 0; cq < 4; ++cq)
        {
          __builtin_amdgcn_raw_buffer_store_b128(__builtin_bit_cast(u32x4, o[4 * tb + cq]), hr, ob + tb * 65536 + cq * 64, 0, 16);
          *LDSP(bf16x8, 65536 + (cq >> 1) * 16384 + (32 * w + 16 * tb + fr) * 128 + (((4 * (cq & 1) + fq) ^ ((fr >> 1) & 7)) * 16)) = o[4 * tb + cq];
        }
    }
    STAMP(6);
    WAIT_V(0);
    if (lane == 0) __hip_atomic_store(&flags[(c * 16 + g) * 16 + w], tag64, __ATOMIC_RELAXED, __HIP_MEMORY_SCOPE_AGENT);
    STAMP(7);
    WAIT_L0();
    BAR();
  }
  const int wd = wave & 1, wt = wave >> 1;
  f32x4 acc[2][4];
#pragma unroll
  for (int i = 0; i < 2; ++i)
#pragma unroll
    for (int j = 0; j < 4; ++j) acc[i][j] = f32x4{0, 0, 0, 0};
  bf16x8 F0[6], F1[6];
  auto rdh = [&](bf16x8* F, int hbase) {
#pragma unroll
    for (int j = 0; j < 4; ++j) F[2 + j] = *LDSP(bf16x8, hbase + (64 * wt + 16 * j) * 128);
  };
  auto rd = [&](bf16x8* F, int hbase, int cbase) {
#pragma unroll
    for (int i = 0; i < 2; ++i) F[i] = *LDSP(bf16x8, cbase + (32 * wd + 16 * i) * 128);
    rdh(F, hbase);
  };
  auto mm = [&](bf16x8* F) {
#pragma unroll
    for (int i = 0; i < 2; ++i)
#pragma unroll
      for (int j = 0; j < 4; ++j) acc[i][j] = __builtin_amdgcn_mfma_f32_16x16x32_bf16(F[i], F[2 + j], acc[i][j], 0, 0, 0);
  };
#pragma unroll
  for (int r = 4; r < 7; ++r)
#pragma unroll
    for (int i = 0; i < 2; ++i) {
      CR[r][0][i] = *LDSP(bf16x8, 40960 + (r - 4) * 8192 + a0 + (32 * wd + 16 * i) * 128);
      CR[r][1][i] = *LDSP(bf16x8, 40960 + (r - 4) * 8192 + a1 + (32 * wd + 16 * i) * 128);
    }
  rd(F0, 65536 + a0, cslot(0) + a0);
  SB0();
  rd(F1, 65536 + a1, cslot(0) + a1); mm(F0); ILV(6, 8)
  rd(F0, 81920 + a0, cslot(1) + a0); mm(F1); ILV(6, 8)
  rd(F1, 81920 + a1, cslot(1) + a1); mm(F0); ILV(6, 8)
  u32x2 xs[2][4], ds[2];
#pragma unroll
  for (int dm = 0; dm < 2; ++dm) {
    int d = 64 * n + 32 * wd + 16 * dm + 4 * fq;
    ds[dm] = *(const u32x2*)(Dskip + d);
#pragma unroll
    for (int tn = 0; tn < 4; ++tn) xs[dm][tn] = *(const u32x2*)(x + (size_t)(128 * c + 64 * wt + 16 * tn + fr) * D + d);
  }
#pragma unroll
  for (int r = 4; r < 7; ++r) asm volatile("" : "+v"(CR[r][0][0]), "+v"(CR[r][0][1]), "+v"(CR[r][1][0]), "+v"(CR[r][1][1]));
  WAIT_L0();
  BAR();
  int hi_ = 0, ci_ = 2;
#pragma unroll 1
  for (int s = 2; s < 25; ++s) {
    int hb = hi_ * 16384, cb = ci_ < 4 ? 131072 + ci_ * 8192 : 98304 + (ci_ - 4) * 8192;
    BAR();
    SB0();
    rd(F0, hb + a0, cb + a0);
    mm(F1);
    ILV(6, 8)
    rd(F1, hb + a1, cb + a1);
    mm(F0);
    ILV(6, 8)
    WAIT_L0();
    hi_ = hi_ == 5 ? 0 : hi_ + 1; ci_ = (ci_ + 1) & 7;
  }
#pragma unroll
  for (int r = 0; r < 7; ++r) {
    const int hb = ((25 + r + 4) % 6) * 16384;
    BAR();
    SB0();
    rdh(F0, hb + a0);
    mm(F1);
    ILV(4, 8)
    F0[0] = CR[r][0][0]; F0[1] = CR[r][0][1];
    rdh(F1, hb + a1);
    mm(F0);
    ILV(4, 8)
    F1[0] = CR[r][1][0]; F1[1] = CR[r][1][1];
    WAIT_L0();
  }
  mm(F1);
  MFMA_DRAIN();
  STAMP(9);
  const __amdgpu_buffer_rsrc_t yr = __builtin_amdgcn_make_buffer_rsrc((void*)y, 0, 2048 * 1024 * 4, 0x00020000);
#pragma unroll
  for (int dm = 0; dm < 2; ++dm) {
    int d = 64 * n + 32 * wd + 16 * dm + 4 * fq;
    float dk[4] = {__uint_as_float(ds[dm][0] << 16), __uint_as_float(ds[dm][0] & 0xffff0000u),
                   __uint_as_float(ds[dm][1] << 16), __uint_as_float(ds[dm][1] & 0xffff0000u)};
#pragma unroll
    for (int tn = 0; tn < 4; ++tn) {
      float xv[4] = {__uint_as_float(xs[dm][tn][0] << 16), __uint_as_float(xs[dm][tn][0] & 0xffff0000u),
                     __uint_as_float(xs[dm][tn][1] << 16), __uint_as_float(xs[dm][tn][1] & 0xffff0000u)};
      f32x4 o;
#pragma unroll
      for (int j = 0; j < 4; ++j) o[j] = 2.0f * acc[dm][tn][j] + bfround(xv[j] * dk[j]);
      __builtin_amdgcn_raw_buffer_store_b128(__builtin_bit_cast(u32x4, o), yr, ((128 * c + 64 * wt + 16 * tn + fr) * D + d) * 4, 0, 16);
    }
  }
}

extern "C" void kernel_launch(void* const* d_in, const int* in_sizes, int n_in, void* d_out, int out_size,
                              void* d_ws, size_t ws_size, hipStream_t stream) {
  const u16* x = (const u16*)d_in[0];
  const u16* Lre = (const u16*)d_in[1];
  const u16* Lim = (const u16*)d_in[2];
  const u16* Bre = (const u16*)d_in[3];
  const u16* Bim = (const u16*)d_in[4];
  const u16* Cre = (const u16*)d_in[5];
  const u16* Cim = (const u16*)d_in[6];
  const u16* logdt = (const u16*)d_in[7];
  const u16* Dskip = (const u16*)d_in[8];
  float* out = (float*)d_out;
  char* ws = (char*)d_ws;
  u32x4* exch = (u32x4*)ws;
  u64* flags = (u64*)(ws + 262144);
  u16* h = (u16*)(ws + (1 << 20));
  (void)hipFuncSetAttribute((const void*)s5_fused, hipFuncAttributeMaxDynamicSharedMemorySize, LDS_BYTES);
  s5_fused<<<256, 512, LDS_BYTES, stream>>>(x, Bre, Bim, Cre, Cim, Lre, Lim, logdt, Dskip, h, exch, flags, out TSPASS);
}
```

```cpp
#include <hip/hip_runtime.h>
#include <hip/hip_bf16.h>
#include <stdint.h>

typedef __attribute__((ext_vector_type(8))) short bf16x8;
typedef __attribute__((ext_vector_type(4))) short s16x4;
typedef __attribute__((ext_vector_type(4))) float f32x4;
typedef __attribute__((ext_vector_type(2))) float f32x2;
typedef __attribute__((ext_vector_type(4))) unsigned u32x4;
typedef __attribute__((ext_vector_type(2))) unsigned u32x2;
typedef unsigned short u16;
typedef unsigned long long u64;
#define LDSP(T, off) ((__attribute__((address_space(3))) T*)(smem + (off)))

#define STAMP(i)
#define LSTAMP(i)
#define TSARG
#define TSPASS

static constexpr int D = 1024, P = 1024;

__device__ __forceinline__ float bf2f(u16 v) { return __uint_as_float(((unsigned)v) << 16); }
__device__ __forceinline__ float bfround(float f) { return __bfloat162float(__float2bfloat16(f)); }
typedef __attribute__((ext_vector_type(2))) __bf16 bf16x2;
__device__ __forceinline__ unsigned pack2(float a, float b) {
  return __builtin_bit_cast(unsigned, __builtin_convertvector(f32x2{a, b}, bf16x2));
}
#define WAIT_V(n) asm volatile("s_waitcnt vmcnt(%0)" ::"i"(n) : "memory")
#define WAIT_L0() asm volatile("s_waitcnt lgkmcnt(0)" ::: "memory")
#define BAR() asm volatile("s_barrier" ::: "memory")
#define SB0() __builtin_amdgcn_sched_barrier(0)
#define MFMA_DRAIN() do { SB0(); asm volatile("s_nop 15\n\ts_nop 15"); SB0(); } while (0)
#define ILV(NR, NM)                                                        \
  _Pragma("unroll") for (int i_ = 0; i_ < NR; ++i_) {                       \
    __builtin_amdgcn_sched_group_barrier(0x8, 1, 0);                        \
    __builtin_amdgcn_sched_group_barrier(0x100, 1, 0);                      \
  }                                                                         \
  __builtin_amdgcn_sched_group_barrier(0x8, NM - NR, 0);                    \
  SB0();
#define GLDS(gp, lo) __builtin_amdgcn_global_load_lds((const unsigned*)(gp), LDSP(unsigned, (lo)), 16, 0, 0)
#define GLDS_SC1(gp, lo) __builtin_amdgcn_global_load_lds((const unsigned*)(gp), LDSP(unsigned, (lo)), 16, 0, 16)

extern "C" __device__ unsigned long long s5_dispatch_id() __asm("llvm.amdgcn.dispatch.id");
__device__ __forceinline__ u32x2 launch_tag() {
  u64 z = s5_dispatch_id() * 0x9E3779B97F4A7C15ull + ((u64)__builtin_amdgcn_dispatch_ptr() >> 6) * 0xD1B54A32D192ED03ull + 0x2545F4914F6CDD1Dull;
  z = (z ^ (z >> 30)) * 0xBF58476D1CE4E5B9ull;
  z = (z ^ (z >> 27)) * 0x94D049BB133111EBull;
  z ^= z >> 31;
  unsigned a = (unsigned)z, b = (unsigned)(z >> 32);
  a = (a & 0xFFFF00FFu) | ((~a & 0xFFu) << 8);
  b = (b & 0xFFFF00FFu) | ((~b & 0xFFu) << 8);
  return u32x2{a, b};
}

__device__ __forceinline__ void sincos_acc(float a, float& sn, float& cs) {
  const double x = (double)a;
  const double k = __builtin_rint(x * 0.63661977236758134308);
  double r = __builtin_fma(-k, 1.57079632679489655800e+00, x);
  r = __builtin_fma(-k, 6.12323399573676603587e-17, r);
  const double z = r * r;
  double ps = __builtin_fma(z, -2.50521083854417187751e-08, 2.75573192239858906526e-06);
  ps = __builtin_fma(z, ps, -1.98412698412698412698e-04);
  ps = __builtin_fma(z, ps, 8.33333333333333333333e-03);
  ps = __builtin_fma(z, ps, -1.66666666666666666667e-01);
  const double sr = __builtin_fma(r * z, ps, r);
  double pc = __builtin_fma(z, 2.08767569878680989792e-09, -2.75573192239858906526e-07);
  pc = __builtin_fma(z, pc, 2.48015873015873015873e-05);
  pc = __builtin_fma(z, pc, -1.38888888888888888889e-03);
  pc = __builtin_fma(z, pc, 4.16666666666666666667e-02);
  pc = __builtin_fma(z, pc, -0.5);
  const double cr = __builtin_fma(z, pc, 1.0);
  const int q = (int)k;
  const float fs = (float)sr, fc = (float)cr;
  const float s0 = (q & 1) ? fc : fs, c0 = (q & 1) ? fs : fc;
  sn = (q & 2) ? -s0 : s0;
  cs = ((q + 1) & 2) ? -c0 : c0;
  if (__builtin_expect(!(__builtin_fabsf(a) < 1.0e6f), 0)) sincosf(a, &sn, &cs);
}

struct cd { double r, i; };
__device__ __forceinline__ cd cmul(cd a, cd b) { return cd{a.r * b.r - a.i * b.i, a.r * b.i + a.i * b.r}; }
__device__ __forceinline__ f32x2 tof(cd a) { return f32x2{(float)a.r, (float)a.i}; }
__device__ __forceinline__ f32x2 cfma(f32x2 a, f32x2 b, f32x2 c) {
  f32x2 t = __builtin_elementwise_fma(f32x2{a.x, a.x}, b, c);
  return __builtin_elementwise_fma(f32x2{-a.y, a.y}, f32x2{b.y, b.x}, t);
}

static constexpr int S1 = 16, ST1 = 32768;
static constexpr int S2 = 32, NS2 = 6;
static constexpr int LDS_BYTES = 163840;
static constexpr int IMG_PITCH = 288;
__device__ __forceinline__ constexpr int cslot(int i) { return i < 4 ? 131072 + i * 8192 : 98304 + (i - 4) * 8192; }

__global__ __launch_bounds__(512, 2) void s5_fused(const u16* __restrict__ x, const u16* __restrict__ Bre,
                                                   const u16* __restrict__ Bim, const u16* __restrict__ Cre,
                                                   const u16* __restrict__ Cim, const u16* __restrict__ Lre,
                                                   const u16* __restrict__ Lim, const u16* __restrict__ logdt,
                                                   const u16* __restrict__ Dskip, u16* h, u32x4* exch,
                                                   u64* flags, float* __restrict__ y TSARG) {
  extern __shared__ __attribute__((aligned(16))) char smem[];
  const int b = blockIdx.x, xcd = b & 7, idx = b >> 3;
  const int c = 4 * (xcd & 3) + (idx & 3), g = 8 * (xcd >> 2) + (idx >> 2);
  const int n = g;
  const int wave = __builtin_amdgcn_readfirstlane(threadIdx.x >> 6), lane = threadIdx.x & 63;
  const u32x2 tag = launch_tag();
  const u64 tag64 = (u64)tag.x | ((u64)tag.y << 32);
  auto coff = [&](int s) { int gb = s < 2 ? g : (((s - 2) >> 1) < g ? ((s - 2) >> 1) : (s >> 1)); return gb * 64; };
  if (wave < 4) {
  STAMP(0);
  const int w = wave, fr = lane & 15, fq = lane >> 4;
  const int c0 = fq ^ (fr >> 1);
  const int a0 = fr * 128 + c0 * 16, a1 = fr * 128 + (c0 ^ 4) * 16;
  bf16x8 CR[7][2][2];
  {
    const int p = 64 * g + 16 * w + fr;
    f32x2 lam, cf, l8, l16, l24, l32, lq, mu4, mue;
    {
      float lr = bf2f(Lre[p]), li = bf2f(Lim[p]);
      float dt = bfround(expf(bf2f(logdt[p])));
      float ar = dt * lr, ai = dt * li;
      float e = expf(ar), sn, cs;
      sincos_acc(ai, sn, cs);
      lam = f32x2{e * cs, e * sn};
      float nr = lam.x - 1.0f, ni = lam.y;
      float den = lr * lr + li * li;
      cf = f32x2{(nr * lr + ni * li) / den, (ni * lr - nr * li) / den};
      lam.y = -lam.y;
      cd a{(double)lam.x, (double)lam.y};
      a = cmul(a, a); a = cmul(a, a); a = cmul(a, a);
      cd a8 = a, a16 = cmul(a8, a8), a24 = cmul(a16, a8), a32 = cmul(a16, a16), a64 = cmul(a32, a32), a96 = cmul(a64, a32);
      cd mu = cmul(a64, a64);
      l8 = tof(a8); l16 = tof(a16); l24 = tof(a24); l32 = tof(a32);
      cd mu2 = cmul(mu, mu), mu3 = cmul(mu2, mu);
      mu4 = tof(cmul(mu2, mu2));
      f32x2 f64_ = tof(a64), f96 = tof(a96), m1 = tof(mu), m2 = tof(mu2), m3 = tof(mu3);
      asm volatile("" : "+v"(f64_), "+v"(f96), "+v"(m1), "+v"(m2), "+v"(m3), "+v"(l32));
      const f32x2 one = {1.f, 0.f};
      const f32x2 qa = (fq & 1) ? l32 : one, qb = (fq & 1) ? f96 : f64_;
      lq = (fq & 2) ? qb : qa;
      const int e2 = (c - 1 - fq) & 3;
      const f32x2 ea = (e2 & 1) ? m1 : one, eb = (e2 & 1) ? m3 : m2;
      mue = (e2 & 2) ? eb : ea;
    }
    asm volatile("" : "+v"(lam), "+v"(cf), "+v"(l8), "+v"(l16), "+v"(l24), "+v"(l32), "+v"(lq), "+v"(mu4), "+v"(mue));
    f32x4 acc[8][2];
#pragma unroll
    for (int m = 0; m < 8; ++m) { acc[m][0] = f32x4{0, 0, 0, 0}; acc[m][1] = f32x4{0, 0, 0, 0}; }
    bf16x8 F0[10], F1[10];
    auto rd = [&](bf16x8* F, int base) {
#pragma unroll
      for (int nn = 0; nn < 2; ++nn) F[8 + nn] = *LDSP(bf16x8, base + 16384 + 4096 * w + 2048 * nn);
#pragma unroll
      for (int m = 0; m < 8; ++m) F[m] = *LDSP(bf16x8, base + 2048 * m);
    };
    auto mm = [&](bf16x8* F) {
#pragma unroll
      for (int m = 0; m < 8; ++m)
#pragma unroll
        for (int nn = 0; nn < 2; ++nn) acc[m][nn] = __builtin_amdgcn_mfma_f32_16x16x32_bf16(F[m], F[8 + nn], acc[m][nn], 0, 0, 0);
    };
    STAMP(1);
    BAR();
    STAMP(2);
    rd(F0, a0);
    SB0();
    rd(F1, a1);
    mm(F0);
    ILV(10, 16)
    WAIT_L0();
    for (int s = 1; s < S1; ++s) {
      int buf = (s & 3) * ST1;
      BAR();
      SB0();
      rd(F0, buf + a0);
      mm(F1);
      ILV(10, 16)
      rd(F1, buf + a1);
      mm(F0);
      ILV(10, 16)
      WAIT_L0();
    }
    mm(F1);
    MFMA_DRAIN();
    STAMP(3);
    f32x2 u[32];
#pragma unroll
    for (int m = 0; m < 8; ++m)
#pragma unroll
      for (int j = 0; j < 4; ++j) {
        float a = acc[m][0][j], bb = acc[m][1][j];
        f32x2 t = f32x2{-cf.y, -cf.x} * f32x2{bb, bb};
        u[4 * m + j] = __builtin_elementwise_fma(f32x2{cf.x, -cf.y}, f32x2{a, a}, t);
      }
    f32x2 ag[4];
#pragma unroll
    for (int k = 0; k < 4; ++k) ag[k] = u[8 * k];
#pragma unroll
    for (int i = 1; i < 8; ++i)
#pragma unroll
      for (int k = 0; k < 4; ++k) ag[k] = cfma(lam, ag[k], u[8 * k + i]);
    f32x2 e1 = ag[0], e2 = cfma(l8, e1, ag[1]), e3 = cfma(l8, e2, ag[2]), la = cfma(l8, e3, ag[3]);
    f32x2 q[4];
#pragma unroll
    for (int k = 0; k < 4; ++k) q[k] = f32x2{__shfl(la.x, fr + 16 * k), __shfl(la.y, fr + 16 * k)};
    f32x2 E1 = q[0], E2 = cfma(l32, E1, q[1]), E3 = cfma(l32, E2, q[2]), AG = cfma(l32, E3, q[3]);
    const __amdgpu_buffer_rsrc_t er = __builtin_amdgcn_make_buffer_rsrc((void*)exch, 0, 16 * P * 16, 0x00020000);
    if (fq == 0 && c < 15)
      __builtin_amdgcn_raw_buffer_store_b128(u32x4{__float_as_uint(AG.x), tag.x, __float_as_uint(AG.y), tag.y}, er, (c * P + p) * 16, 0, 16);
    const f32x2 Ea = (fq & 1) ? E1 : f32x2{0.f, 0.f}, Eb = (fq & 1) ? E3 : E2;
    f32x2 E = (fq & 2) ? Eb : Ea;
    f32x2 car = {0.f, 0.f};
    STAMP(4);
#pragma unroll
    for (int r = 0; r < 4; ++r) {
#pragma unroll
      for (int i = 0; i < 2; ++i) {
        CR[r][0][i] = *LDSP(bf16x8, cslot(r) + a0 + (32 * (w & 1) + 16 * i) * 128);
        CR[r][1][i] = *LDSP(bf16x8, cslot(r) + a1 + (32 * (w & 1) + 16 * i) * 128);
      }
      asm volatile("" : "+v"(CR[r][0][0]), "+v"(CR[r][0][1]), "+v"(CR[r][1][0]), "+v"(CR[r][1][1]));
    }
    if (c > 0) {
      u32x4 v[4];
      for (unsigned spin = 0;; ++spin) {
        bool ok = true;
#pragma unroll
        for (int i = 0; i < 4; ++i) {
          int j = min(fq + 4 * i, c - 1);
          v[i] = __builtin_amdgcn_raw_buffer_load_b128(er, (j * P + p) * 16, 0, 16);
          ok &= (v[i][1] == tag.x) & (v[i][3] == tag.y);
        }
        if (__all(ok) || spin > (1u << 22)) break;
        __builtin_amdgcn_s_sleep(1);
        asm volatile("" ::: "memory");
      }
#pragma unroll
      for (int i = 0; i < 4; ++i) {
        f32x2 vv = {__uint_as_float(v[i][0]), __uint_as_float(v[i][2])};
        if (fq + 4 * i < c) car = cfma(mu4, car, vv);
      }
      car = cfma(mue, car, f32x2{0.f, 0.f});
      car.x += __shfl_xor(car.x, 16); car.y += __shfl_xor(car.y, 16);
      car.x += __shfl_xor(car.x, 32); car.y += __shfl_xor(car.y, 32);
    }
    STAMP(5);
    f32x2 H = cfma(lq, car, E);
    f32x2 hs[4] = {H, cfma(l8, H, e1), cfma(l16, H, e2), cfma(l24, H, e3)};
#pragma unroll
    for (int i = 0; i < 8; ++i)
#pragma unroll
      for (int k = 0; k < 4; ++k) { hs[k] = cfma(lam, hs[k], u[8 * k + i]); u[8 * k + i] = hs[k]; }
#pragma unroll
    for (int i = 0; i < 4; ++i) {
      u32x4 vr, vi;
#pragma unroll
      for (int j = 0; j < 4; ++j) {
        vr[j] = pack2(u[8 * i + 2 * j].x, u[8 * i + 2 * j + 1].x);
        vi[j] = pack2(u[8 * i + 2 * j].y, u[8 * i + 2 * j + 1].y);
      }
      *LDSP(u32x4, (16 * w + fr) * IMG_PITCH + 64 * fq + 16 * i) = vr;
      *LDSP(u32x4, (64 + 16 * w + fr) * IMG_PITCH + 64 * fq + 16 * i) = vi;
    }
    WAIT_L0();
    BAR();
    {
      const __amdgpu_buffer_rsrc_t hr = __builtin_amdgcn_make_buffer_rsrc((void*)h, 0, 2048 * 2048 * 2, 0x00020000);
      const int a = (lane >> 2) & 3, bb = lane & 3;
      const int ib = (8 * fq + a) * IMG_PITCH + (32 * w + 4 * bb) * 2;
      bf16x8 o[8];
#pragma unroll
      for (int tb = 0; tb < 2; ++tb)
#pragma unroll
        for (int cq = 0; cq < 4; ++cq) {
          s16x4 lo = __builtin_amdgcn_ds_read_tr16_b64_v4i16(LDSP(s16x4, ib + (32 * cq) * IMG_PITCH + 32 * tb));
          s16x4 hi = __builtin_amdgcn_ds_read_tr16_b64_v4i16(LDSP(s16x4, ib + (32 * cq + 4) * IMG_PITCH + 32 * tb));
          o[4 * tb + cq] = __builtin_shufflevector(lo, hi, 0, 1, 2, 3, 4, 5, 6, 7);
        }
      SB0();
      const int ob = ((128 * c + 32 * w + fr) * 2048 + 128 * g + 8 * fq) * 2;
#pragma unroll
      for (int tb = 0; tb < 2; ++tb)
#pragma unroll
        for (int cq = 0; cq < 4; ++cq)
        {
          __builtin_amdgcn_raw_buffer_store_b128(__builtin_bit_cast(u32x4, o[4 * tb + cq]), hr, ob + tb * 65536 + cq * 64, 0, 16);
          *LDSP(bf16x8, 65536 + (cq >> 1) * 16384 + (32 * w + 16 * tb + fr) * 128 + (((4 * (cq & 1) + fq) ^ ((fr >> 1) & 7)) * 16)) = o[4 * tb + cq];
        }
    }
    STAMP(6);
    WAIT_V(0);
    if (lane == 0) __hip_atomic_store(&flags[(c * 16 + g) * 16 + w], tag64, __ATOMIC_RELAXED, __HIP_MEMORY_SCOPE_AGENT);
    STAMP(7);
    WAIT_L0();
    BAR();
  }
  const int wd = wave & 1, wt = wave >> 1;
  f32x4 acc[2][4];
#pragma unroll
  for (int i = 0; i < 2; ++i)
#pragma unroll
    for (int j = 0; j < 4; ++j) acc[i][j] = f32x4{0, 0, 0, 0};
  bf16x8 F0[6], F1[6];
  auto rdh = [&](bf16x8* F, int hbase) {
#pragma unroll
    for (int j = 0; j < 4; ++j) F[2 + j] = *LDSP(bf16x8, hbase + (64 * wt + 16 * j) * 128);
  };
  auto rd = [&](bf16x8* F, int hbase, int cbase) {
#pragma unroll
    for (int i = 0; i < 2; ++i) F[i] = *LDSP(bf16x8, cbase + (32 * wd + 16 * i) * 128);
    rdh(F, hbase);
  };
  auto mm = [&](bf16x8* F) {
#pragma unroll
    for (int i = 0; i < 2; ++i)
#pragma unroll
      for (int j = 0; j < 4; ++j) acc[i][j] = __builtin_amdgcn_mfma_f32_16x16x32_bf16(F[i], F[2 + j], acc[i][j], 0, 0, 0);
  };
#pragma unroll
  for (int r = 4; r < 7; ++r)
#pragma unroll
    for (int i = 0; i < 2; ++i) {
      CR[r][0][i] = *LDSP(bf16x8, 40960 + (r - 4) * 8192 + a0 + (32 * wd + 16 * i) * 128);
      CR[r][1][i] = *LDSP(bf16x8, 40960 + (r - 4) * 8192 + a1 + (32 * wd + 16 * i) * 128);
    }
  rd(F0, 65536 + a0, cslot(0) + a0);
  SB0();
  rd(F1, 65536 + a1, cslot(0) + a1); mm(F0); ILV(6, 8)
  rd(F0, 81920 + a0, cslot(1) + a0); mm(F1); ILV(6, 8)
  rd(F1, 81920 + a1, cslot(1) + a1); mm(F0); ILV(6, 8)
  u32x2 xs[2][4], ds[2];
#pragma unroll
  for (int dm = 0; dm < 2; ++dm) {
    int d = 64 * n + 32 * wd + 16 * dm + 4 * fq;
    ds[dm] = *(const u32x2*)(Dskip + d);
#pragma unroll
    for (int tn = 0; tn < 4; ++tn) xs[dm][tn] = *(const u32x2*)(x + (size_t)(128 * c + 64 * wt + 16 * tn + fr) * D + d);
  }
#pragma unroll
  for (int r = 4; r < 7; ++r) asm volatile("" : "+v"(CR[r][0][0]), "+v"(CR[r][0][1]), "+v"(CR[r][1][0]), "+v"(CR[r][1][1]));
  WAIT_L0();
  BAR();
  int hi_ = 0, ci_ = 2;
#pragma unroll 1
  for (int s = 2; s < 25; ++s) {
    int hb = hi_ * 16384, cb = ci_ < 4 ? 131072 + ci_ * 8192 : 98304 + (ci_ - 4) * 8192;
    BAR();
    SB0();
    rd(F0, hb + a0, cb + a0);
    mm(F1);
    ILV(6, 8)
    rd(F1, hb + a1, cb + a1);
    mm(F0);
    ILV(6, 8)
    WAIT_L0();
    hi_ = hi_ == 5 ? 0 : hi_ + 1; ci_ = (ci_ + 1) & 7;
  }
#pragma unroll
  for (int r = 0; r < 7; ++r) {
    const int hb = ((25 + r + 4) % 6) * 16384;
    BAR();
    SB0();
    rdh(F0, hb + a0);
    mm(F1);
    ILV(4, 8)
    F0[0] = CR[r][0][0]; F0[1] = CR[r][0][1];
    rdh(F1, hb + a1);
    mm(F0);
    ILV(4, 8)
    F1[0] = CR[r][1][0]; F1[1] = CR[r][1][1];
    WAIT_L0();
  }
  mm(F1);
  MFMA_DRAIN();
  STAMP(9);
  const __amdgpu_buffer_rsrc_t yr = __builtin_amdgcn_make_buffer_rsrc((void*)y, 0, 2048 * 1024 * 4, 0x00020000);
#pragma unroll
  for (int dm = 0; dm < 2; ++dm) {
    int d = 64 * n + 32 * wd + 16 * dm + 4 * fq;
    float dk[4] = {__uint_as_float(ds[dm][0] << 16), __uint_as_float(ds[dm][0] & 0xffff0000u),
                   __uint_as_float(ds[dm][1] << 16), __uint_as_float(ds[dm][1] & 0xffff0000u)};
#pragma unroll
    for (int tn = 0; tn < 4; ++tn) {
      float xv[4] = {__uint_as_float(xs[dm][tn][0] << 16), __uint_as_float(xs[dm][tn][0] & 0xffff0000u),
                     __uint_as_float(xs[dm][tn][1] << 16), __uint_as_float(xs[dm][tn][1] & 0xffff0000u)};
      f32x4 o;
#pragma unroll
      for (int j = 0; j < 4; ++j) o[j] = 2.0f * acc[dm][tn][j] + bfround(xv[j] * dk[j]);
      __builtin_amdgcn_raw_buffer_store_b128(__builtin_bit_cast(u32x4, o), yr, ((128 * c + 64 * wt + 16 * tn + fr) * D + d) * 4, 0, 16);
    }
  }
  return;
  }
  {
    const int lw = wave - 4;
    int offC[2];
#pragma unroll
    for (int i = 0; i < 2; ++i) {
      int rho = 16 * lw + 8 * i + (lane >> 3);
      int chunk = (lane & 7) ^ ((rho >> 1) & 7);
      offC[i] = (64 * n + rho) * P + chunk * 8;
    }
    auto issueC = [&](int s, int slot_off) {
      const u16* Cb = ((s & 1) ? Cim : Cre) + coff(s);
#pragma unroll
      for (int i = 0; i < 2; ++i) GLDS(Cb + offC[i], slot_off + (2 * lw + i) * 1024);
    };
    {
      const u16* src[8];
#pragma unroll
      for (int i = 0; i < 4; ++i) {
        int rho = 32 * lw + 8 * i + (lane >> 3);
        int chunk = (lane & 7) ^ ((rho >> 1) & 7);
        int m = rho >> 4, i16 = rho & 15;
        int tau = 32 * (i16 >> 2) + 4 * m + (i16 & 3);
        src[i] = x + (size_t)(128 * c + tau) * D + chunk * 8;
        int p = 64 * g + 16 * (rho >> 5) + (rho & 15);
        src[4 + i] = ((rho & 16) ? Bim : Bre) + (size_t)p * D + chunk * 8;
      }
      const int koff = 4 * (((idx >> 2) + idx) & 3);
      auto issue = [&](int s0) {
        int buf = (s0 & 3) * ST1;
        int s = (s0 + koff) & 15;
#pragma unroll
        for (int i = 0; i < 4; ++i) GLDS(src[i] + s * 64, buf + (4 * lw + i) * 1024);
#pragma unroll
        for (int i = 0; i < 4; ++i) GLDS(src[4 + i] + s * 64, buf + 16384 + (4 * lw + i) * 1024);
      };
      issue(0); issue(1); issue(2);
      for (int s = 0; s < 6; ++s) { WAIT_V(16); BAR(); issue(s + 3); }
      WAIT_V(16); BAR(); issue(9);
      {
        const char* p1 = (const char*)Cre + b * 8192 + lw * 2048 + lane * 16;
        const char* p2 = (const char*)Cim + b * 8192 + lw * 2048 + lane * 16;
        GLDS(p1, 131072 + lw * 1024); GLDS(p1 + 1024, 131072 + lw * 1024);
        GLDS(p2, 131072 + lw * 1024); GLDS(p2 + 1024, 131072 + lw * 1024);
      }
      WAIT_V(20); BAR(); issue(10);
      WAIT_V(20); BAR(); issue(11);
      WAIT_V(20); BAR(); issue(12);
      WAIT_V(16); BAR(); issue(13);
      WAIT_V(16); BAR(); issue(14);
      WAIT_V(16); BAR(); issue(15);
      issueC(25, cslot(0)); issueC(26, cslot(1)); issueC(27, cslot(2)); issueC(28, cslot(3));
      WAIT_V(24); BAR();
      WAIT_V(16); BAR();
      WAIT_V(0); BAR();
    }
    const u16* srcH[4];
#pragma unroll
    for (int i = 0; i < 4; ++i) {
      int rho = 32 * lw + 8 * i + (lane >> 3);
      int chunk = (lane & 7) ^ ((rho >> 1) & 7);
      srcH[i] = h + (size_t)(128 * c + rho) * 2048 + chunk * 8;
    }
    auto issueH = [&](int s, int bi) {
      const int ho = coff(s) * 2 + (s & 1) * 64;
#pragma unroll
      for (int i = 0; i < 4; ++i) GLDS_SC1(srcH[i] + ho, bi * 16384 + (4 * lw + i) * 1024);
    };
    issueC(29, 40960); issueC(30, 49152); issueC(31, 57344);
    BAR();
    issueC(0, cslot(0)); issueC(1, cslot(1));
    WAIT_V(0);
    BAR();
    if (lw == 0) {
      for (unsigned spin = 0;; ++spin) {
        u64 v = __hip_atomic_load(&flags[(c * 16 + (lane >> 2)) * 16 + (lane & 3)], __ATOMIC_RELAXED, __HIP_MEMORY_SCOPE_AGENT);
        if (__all(v == tag64) || spin > (1u << 22)) break;
        __builtin_amdgcn_s_sleep(2);
      }
    }
#pragma unroll
    for (int k = 2; k < 8; ++k) issueC(k, cslot(k));
    BAR();
    issueH(2, 0);
    issueH(3, 1);
    issueH(4, 2);
    issueH(5, 3);
    WAIT_V(12); BAR();
    issueH(6, 4);
    issueC(8, cslot(0));
    issueH(7, 5);
    issueC(9, cslot(1));
    WAIT_V(20); BAR();
    issueH(8, 0);
    issueC(10, cslot(2));
    WAIT_V(22); BAR();
    issueH(9, 1);
    issueC(11, cslot(3));
    WAIT_V(24); BAR();
    issueH(10, 2);
    issueC(12, cslot(4));
    WAIT_V(26); BAR();
    issueH(11, 3);
    issueC(13, cslot(5));
    WAIT_V(26); BAR();
    issueH(12, 4);
    issueC(14, cslot(6));
    WAIT_V(26); BAR();
    issueH(13, 5);
    issueC(15, cslot(7));
    WAIT_V(26); BAR();
    issueH(14, 0);
    issueC(16, cslot(0));
    WAIT_V(26); BAR();
    issueH(15, 1);
    issueC(17, cslot(1));
    WAIT_V(26); BAR();
    issueH(16, 2);
    issueC(18, cslot(2));
    WAIT_V(26); BAR();
    issueH(17, 3);
    issueC(19, cslot(3));
    WAIT_V(26); BAR();
    issueH(18, 4);
    issueC(20, cslot(4));
    WAIT_V(26); BAR();
    issueH(19, 5);
    issueC(21, cslot(5));
    WAIT_V(26); BAR();
    issueH(20, 0);
    issueC(22, cslot(6));
    WAIT_V(26); BAR();
    issueH(21, 1);
    issueC(23, cslot(7));
    WAIT_V(26); BAR();
    issueH(22, 2);
    issueC(24, cslot(0));
    WAIT_V(26); BAR();
    issueH(23, 3);
    WAIT_V(24); BAR();
    issueH(24, 4);
    WAIT_V(22); BAR();
    issueH(25, 5);
    WAIT_V(20); BAR();
    issueH(26, 0);
    WAIT_V(18); BAR();
    issueH(27, 1);
    WAIT_V(16); BAR();
    issueH(28, 2);
    WAIT_V(16); BAR();
    issueH(29, 3);
    WAIT_V(16); BAR();
    issueH(30, 4);
    WAIT_V(16); BAR();
    issueH(31, 5);
    WAIT_V(16); BAR();
    WAIT_V(12); BAR();
    WAIT_V(8); BAR();
    WAIT_V(4); BAR();
    WAIT_V(0); BAR();
    return;
  }
}

extern "C" void kernel_launch(void* const* d_in, const int* in_sizes, int n_in, void* d_out, int out_size,
                              void* d_ws, size_t ws_size, hipStream_t stream) {
  const u16* x = (const u16*)d_in[0];
  const u16* Lre = (const u16*)d_in[1];
  const u16* Lim = (const u16*)d_in[2];
  const u16* Bre = (const u16*)d_in[3];
  const u16* Bim = (const u16*)d_in[4];
  const u16* Cre = (const u16*)d_in[5];
  const u16* Cim = (const u16*)d_in[6];
  const u16* logdt = (const u16*)d_in[7];
  const u16* Dskip = (const u16*)d_in[8];
  float* out = (float*)d_out;
  char* ws = (char*)d_ws;
  u32x4* exch = (u32x4*)ws;
  u64* flags = (u64*)(ws + 262144);
  u16* h = (u16*)(ws + (1 << 20));
  (void)hipFuncSetAttribute((const void*)s5_fused, hipFuncAttributeMaxDynamicSharedMemorySize, LDS_BYTES);
  s5_fused<<<256, 512, LDS_BYTES, stream>>>(x, Bre, Bim, Cre, Cim, Lre, Lim, logdt, Dskip, h, exch, flags, out TSPASS);
}
```

```cpp
#include <hip/hip_runtime.h>
#include <hip/hip_bf16.h>
#include <stdint.h>

typedef __attribute__((ext_vector_type(8))) short bf16x8;
typedef __attribute__((ext_vector_type(4))) short s16x4;
typedef __attribute__((ext_vector_type(4))) float f32x4;
typedef __attribute__((ext_vector_type(2))) float f32x2;
typedef __attribute__((ext_vector_type(4))) unsigned u32x4;
typedef __attribute__((ext_vector_type(2))) unsigned u32x2;
typedef unsigned short u16;
typedef unsigned long long u64;
#define LDSP(T, off) ((__attribute__((address_space(3))) T*)(smem + (off)))

#define STAMP(i)
#define LSTAMP(i)
#define TSARG
#define TSPASS

static constexpr int D = 1024, P = 1024;

__device__ __forceinline__ float bf2f(u16 v) { return __uint_as_float(((unsigned)v) << 16); }
__device__ __forceinline__ float bfround(float f) { return __bfloat162float(__float2bfloat16(f)); }
typedef __attribute__((ext_vector_type(2))) __bf16 bf16x2;
__device__ __forceinline__ unsigned pack2(float a, float b) {
  return __builtin_bit_cast(unsigned, __builtin_convertvector(f32x2{a, b}, bf16x2));
}
#define WAIT_V(n) asm volatile("s_waitcnt vmcnt(%0)" ::"i"(n) : "memory")
#define WAIT_L0() asm volatile("s_waitcnt lgkmcnt(0)" ::: "memory")
#define BAR() asm volatile("s_barrier" ::: "memory")
#define SB0() __builtin_amdgcn_sched_barrier(0)
#define MFMA_DRAIN() do { SB0(); asm volatile("s_nop 15\n\ts_nop 15"); SB0(); } while (0)
#define ILV(NR, NM)                                                        \
  _Pragma("unroll") for (int i_ = 0; i_ < NR; ++i_) {                       \
    __builtin_amdgcn_sched_group_barrier(0x8, 1, 0);                        \
    __builtin_amdgcn_sched_group_barrier(0x100, 1, 0);                      \
  }                                                                         \
  __builtin_amdgcn_sched_group_barrier(0x8, NM - NR, 0);                    \
  SB0();
#define GLDS(gp, lo) __builtin_amdgcn_global_load_lds((const unsigned*)(gp), LDSP(unsigned, (lo)), 16, 0, 0)
#define GLDS_SC1(gp, lo) __builtin_amdgcn_global_load_lds((const unsigned*)(gp), LDSP(unsigned, (lo)), 16, 0, 16)

extern "C" __device__ unsigned long long s5_dispatch_id() __asm("llvm.amdgcn.dispatch.id");
__device__ __forceinline__ u32x2 launch_tag() {
  u64 z = s5_dispatch_id() * 0x9E3779B97F4A7C15ull + ((u64)__builtin_amdgcn_dispatch_ptr() >> 6) * 0xD1B54A32D192ED03ull + 0x2545F4914F6CDD1Dull;
  z = (z ^ (z >> 30)) * 0xBF58476D1CE4E5B9ull;
  z = (z ^ (z >> 27)) * 0x94D049BB133111EBull;
  z ^= z >> 31;
  unsigned a = (unsigned)z, b = (unsigned)(z >> 32);
  a = (a & 0xFFFF00FFu) | ((~a & 0xFFu) << 8);
  b = (b & 0xFFFF00FFu) | ((~b & 0xFFu) << 8);
  return u32x2{a, b};
}

__device__ __forceinline__ void sincos_acc(float a, float& sn, float& cs) {
  const double x = (double)a;
  const double k = __builtin_rint(x * 0.63661977236758134308);
  double r = __builtin_fma(-k, 1.57079632679489655800e+00, x);
  r = __builtin_fma(-k, 6.12323399573676603587e-17, r);
  const double z = r * r;
  double ps = __builtin_fma(z, -2.50521083854417187751e-08, 2.75573192239858906526e-06);
  ps = __builtin_fma(z, ps, -1.98412698412698412698e-04);
  ps = __builtin_fma(z, ps, 8.33333333333333333333e-03);
  ps = __builtin_fma(z, ps, -1.66666666666666666667e-01);
  const double sr = __builtin_fma(r * z, ps, r);
  double pc = __builtin_fma(z, 2.08767569878680989792e-09, -2.75573192239858906526e-07);
  pc = __builtin_fma(z, pc, 2.48015873015873015873e-05);
  pc = __builtin_fma(z, pc, -1.38888888888888888889e-03);
  pc = __builtin_fma(z, pc, 4.16666666666666666667e-02);
  pc = __builtin_fma(z, pc, -0.5);
  const double cr = __builtin_fma(z, pc, 1.0);
  const int q = (int)k;
  const float fs = (float)sr, fc = (float)cr;
  const float s0 = (q & 1) ? fc : fs, c0 = (q & 1) ? fs : fc;
  sn = (q & 2) ? -s0 : s0;
  cs = ((q + 1) & 2) ? -c0 : c0;
  if (__builtin_expect(!(__builtin_fabsf(a) < 1.0e6f), 0)) sincosf(a, &sn, &cs);
}

struct cd { double r, i; };
__device__ __forceinline__ cd cmul(cd a, cd b) { return cd{a.r * b.r - a.i * b.i, a.r * b.i + a.i * b.r}; }
__device__ __forceinline__ f32x2 tof(cd a) { return f32x2{(float)a.r, (float)a.i}; }
__device__ __forceinline__ f32x2 cfma(f32x2 a, f32x2 b, f32x2 c) {
  f32x2 t = __builtin_elementwise_fma(f32x2{a.x, a.x}, b, c);
  return __builtin_elementwise_fma(f32x2{-a.y, a.y}, f32x2{b.y, b.x}, t);
}

static constexpr int S1 = 16, ST1 = 32768;
static constexpr int S2 = 32, NS2 = 6;
static constexpr int LDS_BYTES = 163840;
static constexpr int IMG_PITCH = 288;
__device__ __forceinline__ constexpr int cslot(int i) { return i < 4 ? 131072 + i * 8192 : 98304 + (i - 4) * 8192; }

__global__ __launch_bounds__(512, 2) void s5_fused(const u16* __restrict__ x, const u16* __restrict__ Bre,
                                                   const u16* __restrict__ Bim, const u16* __restrict__ Cre,
                                                   const u16* __restrict__ Cim, const u16* __restrict__ Lre,
                                                   const u16* __restrict__ Lim, const u16* __restrict__ logdt,
                                                   const u16* __restrict__ Dskip, u16* h, u32x4* exch,
                                                   u64* flags, float* __restrict__ y TSARG) {
  extern __shared__ __attribute__((aligned(16))) char smem[];
  const int b = blockIdx.x, xcd = b & 7, idx = b >> 3;
  const int c = 4 * (xcd & 3) + (idx & 3), g = 8 * (xcd >> 2) + (idx >> 2);
  const int n = g;
  const int wave = __builtin_amdgcn_readfirstlane(threadIdx.x >> 6), lane = threadIdx.x & 63;
  const int koff = 4 * (((idx >> 2) + idx) & 3);
  const u32x2 tag = launch_tag();
  const u64 tag64 = (u64)tag.x | ((u64)tag.y << 32);
  auto coff = [&](int s) { int gb = s < 2 ? g : (((s - 2) >> 1) < g ? ((s - 2) >> 1) : (s >> 1)); return gb * 64; };
  if (wave < 4) {
  STAMP(0);
  const int w = wave, fr = lane & 15, fq = lane >> 4;
  const int c0 = fq ^ (fr >> 1);
  const int a0 = fr * 128 + c0 * 16, a1 = fr * 128 + (c0 ^ 4) * 16;
  bf16x8 CR[7][2][2];
  const int wd = wave & 1, wt = wave >> 1;
  u32x2 xs[2][4], ds[2];
  ds[0] = *(const u32x2*)(Dskip + 64 * n + 32 * wd + 4 * fq);
  ds[1] = *(const u32x2*)(Dskip + 64 * n + 32 * wd + 16 + 4 * fq);
  {
    const int p = 64 * g + 16 * w + fr;
    f32x2 lam, cf, l8, l16, l24, l32, lq, mu4, mue;
    {
      float lr = bf2f(Lre[p]), li = bf2f(Lim[p]);
      float dt = bfround(expf(bf2f(logdt[p])));
      float ar = dt * lr, ai = dt * li;
      float e = expf(ar), sn, cs;
      sincos_acc(ai, sn, cs);
      lam = f32x2{e * cs, e * sn};
      float nr = lam.x - 1.0f, ni = lam.y;
      float den = lr * lr + li * li;
      cf = f32x2{(nr * lr + ni * li) / den, (ni * lr - nr * li) / den};
      lam.y = -lam.y;
      cd a{(double)lam.x, (double)lam.y};
      a = cmul(a, a); a = cmul(a, a); a = cmul(a, a);
      cd a8 = a, a16 = cmul(a8, a8), a24 = cmul(a16, a8), a32 = cmul(a16, a16), a64 = cmul(a32, a32), a96 = cmul(a64, a32);
      cd mu = cmul(a64, a64);
      l8 = tof(a8); l16 = tof(a16); l24 = tof(a24); l32 = tof(a32);
      cd mu2 = cmul(mu, mu), mu3 = cmul(mu2, mu);
      mu4 = tof(cmul(mu2, mu2));
      f32x2 f64_ = tof(a64), f96 = tof(a96), m1 = tof(mu), m2 = tof(mu2), m3 = tof(mu3);
      asm volatile("" : "+v"(f64_), "+v"(f96), "+v"(m1), "+v"(m2), "+v"(m3), "+v"(l32));
      const f32x2 one = {1.f, 0.f};
      const f32x2 qa = (fq & 1) ? l32 : one, qb = (fq & 1) ? f96 : f64_;
      lq = (fq & 2) ? qb : qa;
      const int e2 = (c - 1 - fq) & 3;
      const f32x2 ea = (e2 & 1) ? m1 : one, eb = (e2 & 1) ? m3 : m2;
      mue = (e2 & 2) ? eb : ea;
    }
    asm volatile("" : "+v"(lam), "+v"(cf), "+v"(l8), "+v"(l16), "+v"(l24), "+v"(l32), "+v"(lq), "+v"(mu4), "+v"(mue), "+v"(ds[0]), "+v"(ds[1]));
    f32x4 acc[8][2];
#pragma unroll
    for (int m = 0; m < 8; ++m) { acc[m][0] = f32x4{0, 0, 0, 0}; acc[m][1] = f32x4{0, 0, 0, 0}; }
    bf16x8 F0[10], F1[10];
    auto rd = [&](bf16x8* F, int base) {
#pragma unroll
      for (int nn = 0; nn < 2; ++nn) F[8 + nn] = *LDSP(bf16x8, base + 16384 + 4096 * w + 2048 * nn);
#pragma unroll
      for (int m = 0; m < 8; ++m) F[m] = *LDSP(bf16x8, base + 2048 * m);
    };
    auto mm = [&](bf16x8* F) {
#pragma unroll
      for (int m = 0; m < 8; ++m)
#pragma unroll
        for (int nn = 0; nn < 2; ++nn) acc[m][nn] = __builtin_amdgcn_mfma_f32_16x16x32_bf16(F[m], F[8 + nn], acc[m][nn], 0, 0, 0);
    };
    STAMP(1);
    BAR();
    STAMP(2);
    rd(F0, a0);
    SB0();
    rd(F1, a1);
    mm(F0);
    ILV(10, 16)
    WAIT_L0();
    const int sx = (n - koff) & 15;
    int s = 1;
#pragma unroll 1
    for (int part = 0; part < 2; ++part) {
      const int hi = part ? S1 : sx + 1;
#pragma unroll 1
      for (; s < hi; ++s) {
      int buf = (s & 3) * ST1;
      BAR();
      SB0();
      rd(F0, buf + a0);
      mm(F1);
      ILV(10, 16)
      rd(F1, buf + a1);
      mm(F0);
      ILV(10, 16)
      WAIT_L0();
      }
      if (part == 0) {
#pragma unroll
        for (int tn = 0; tn < 4; ++tn) {
          const int tau = 64 * wt + 16 * tn + fr;
          const int rho = 16 * ((tau >> 2) & 7) + 4 * (tau >> 5) + (tau & 3);
#pragma unroll
          for (int dm = 0; dm < 2; ++dm)
            xs[dm][tn] = *LDSP(u32x2, (sx & 3) * ST1 + rho * 128 + (((4 * wd + 2 * dm + (fq >> 1)) ^ ((rho >> 1) & 7)) * 16) + (fq & 1) * 8);
        }
        WAIT_L0();
        SB0();
      }
    }
    BAR();
    mm(F1);
    MFMA_DRAIN();
    STAMP(3);
    f32x2 u[32];
#pragma unroll
    for (int m = 0; m < 8; ++m)
#pragma unroll
      for (int j = 0; j < 4; ++j) {
        float a = acc[m][0][j], bb = acc[m][1][j];
        f32x2 t = f32x2{-cf.y, -cf.x} * f32x2{bb, bb};
        u[4 * m + j] = __builtin_elementwise_fma(f32x2{cf.x, -cf.y}, f32x2{a, a}, t);
      }
    f32x2 ag[4];
#pragma unroll
    for (int k = 0; k < 4; ++k) ag[k] = u[8 * k];
#pragma unroll
    for (int i = 1; i < 8; ++i)
#pragma unroll
      for (int k = 0; k < 4; ++k) ag[k] = cfma(lam, ag[k], u[8 * k + i]);
    f32x2 e1 = ag[0], e2 = cfma(l8, e1, ag[1]), e3 = cfma(l8, e2, ag[2]), la = cfma(l8, e3, ag[3]);
    f32x2 q[4];
#pragma unroll
    for (int k = 0; k < 4; ++k) q[k] = f32x2{__shfl(la.x, fr + 16 * k), __shfl(la.y, fr + 16 * k)};
    f32x2 E1 = q[0], E2 = cfma(l32, E1, q[1]), E3 = cfma(l32, E2, q[2]), AG = cfma(l32, E3, q[3]);
    const __amdgpu_buffer_rsrc_t er = __builtin_amdgcn_make_buffer_rsrc((void*)exch, 0, 16 * P * 16, 0x00020000);
    if (fq == 0 && c < 15)
      __builtin_amdgcn_raw_buffer_store_b128(u32x4{__float_as_uint(AG.x), tag.x, __float_as_uint(AG.y), tag.y}, er, (c * P + p) * 16, 0, 16);
    const f32x2 Ea = (fq & 1) ? E1 : f32x2{0.f, 0.f}, Eb = (fq & 1) ? E3 : E2;
    f32x2 E = (fq & 2) ? Eb : Ea;
    f32x2 car = {0.f, 0.f};
    STAMP(4);
#pragma unroll
    for (int r = 0; r < 4; ++r) {
#pragma unroll
      for (int i = 0; i < 2; ++i) {
        CR[r][0][i] = *LDSP(bf16x8, cslot(r) + a0 + (32 * (w & 1) + 16 * i) * 128);
        CR[r][1][i] = *LDSP(bf16x8, cslot(r) + a1 + (32 * (w & 1) + 16 * i) * 128);
      }
      asm volatile("" : "+v"(CR[r][0][0]), "+v"(CR[r][0][1]), "+v"(CR[r][1][0]), "+v"(CR[r][1][1]));
    }
    if (c > 0) {
      u32x4 v[4];
      for (unsigned spin = 0;; ++spin) {
        bool ok = true;
#pragma unroll
        for (int i = 0; i < 4; ++i) {
          int j = min(fq + 4 * i, c - 1);
          v[i] = __builtin_amdgcn_raw_buffer_load_b128(er, (j * P + p) * 16, 0, 16);
          ok &= (v[i][1] == tag.x) & (v[i][3] == tag.y);
        }
        if (__all(ok) || spin > (1u << 22)) break;
        __builtin_amdgcn_s_sleep(1);
        asm volatile("" ::: "memory");
      }
#pragma unroll
      for (int i = 0; i < 4; ++i) {
        f32x2 vv = {__uint_as_float(v[i][0]), __uint_as_float(v[i][2])};
        if (fq + 4 * i < c) car = cfma(mu4, car, vv);
      }
      car = cfma(mue, car, f32x2{0.f, 0.f});
      car.x += __shfl_xor(car.x, 16); car.y += __shfl_xor(car.y, 16);
      car.x += __shfl_xor(car.x, 32); car.y += __shfl_xor(car.y, 32);
    }
    STAMP(5);
    f32x2 H = cfma(lq, car, E);
    f32x2 hs[4] = {H, cfma(l8, H, e1), cfma(l16, H, e2), cfma(l24, H, e3)};
#pragma unroll
    for (int i = 0; i < 8; ++i)
#pragma unroll
      for (int k = 0; k < 4; ++k) { hs[k] = cfma(lam, hs[k], u[8 * k + i]); u[8 * k + i] = hs[k]; }
#pragma unroll
    for (int i = 0; i < 4; ++i) {
      u32x4 vr, vi;
#pragma unroll
      for (int j = 0; j < 4; ++j) {
        vr[j] = pack2(u[8 * i + 2 * j].x, u[8 * i + 2 * j + 1].x);
        vi[j] = pack2(u[8 * i + 2 * j].y, u[8 * i + 2 * j + 1].y);
      }
      *LDSP(u32x4, (16 * w + fr) * IMG_PITCH + 64 * fq + 16 * i) = vr;
      *LDSP(u32x4, (64 + 16 * w + fr) * IMG_PITCH + 64 * fq + 16 * i) = vi;
    }
    WAIT_L0();
    BAR();
    {
      const __amdgpu_buffer_rsrc_t hr = __builtin_amdgcn_make_buffer_rsrc((void*)h, 0, 2048 * 2048 * 2, 0x00020000);
      const int a = (lane >> 2) & 3, bb = lane & 3;
      const int ib = (8 * fq + a) * IMG_PITCH + (32 * w + 4 * bb) * 2;
      bf16x8 o[8];
#pragma unroll
      for (int tb = 0; tb < 2; ++tb)
#pragma unroll
        for (int cq = 0; cq < 4; ++cq) {
          s16x4 lo = __builtin_amdgcn_ds_read_tr16_b64_v4i16(LDSP(s16x4, ib + (32 * cq) * IMG_PITCH + 32 * tb));
          s16x4 hi = __builtin_amdgcn_ds_read_tr16_b64_v4i16(LDSP(s16x4, ib + (32 * cq + 4) * IMG_PITCH + 32 * tb));
          o[4 * tb + cq] = __builtin_shufflevector(lo, hi, 0, 1, 2, 3, 4, 5, 6, 7);
        }
      SB0();
      const int ob = ((128 * c + 32 * w + fr) * 2048 + 128 * g + 8 * fq) * 2;
#pragma unroll
      for (int tb = 0; tb < 2; ++tb)
#pragma unroll
        for (int cq = 0; cq < 4; ++cq)
        {
          __builtin_amdgcn_raw_buffer_store_b128(__builtin_bit_cast(u32x4, o[4 * tb + cq]), hr, ob + tb * 65536 + cq * 64, 0, 16);
          *LDSP(bf16x8, 65536 + (cq >> 1) * 16384 + (32 * w + 16 * tb + fr) * 128 + (((4 * (cq & 1) + fq) ^ ((fr >> 1) & 7)) * 16)) = o[4 * tb + cq];
        }
    }
    STAMP(6);
    WAIT_V(0);
    if (lane == 0) __hip_atomic_store(&flags[(c * 16 + g) * 16 + w], tag64, __ATOMIC_RELAXED, __HIP_MEMORY_SCOPE_AGENT);
    STAMP(7);
    WAIT_L0();
    BAR();
  }
  f32x4 acc[2][4];
#pragma unroll
  for (int i = 0; i < 2; ++i)
#pragma unroll
    for (int j = 0; j < 4; ++j) acc[i][j] = f32x4{0, 0, 0, 0};
  bf16x8 F0[6], F1[6];
  auto rdh = [&](bf16x8* F, int hbase) {
#pragma unroll
    for (int j = 0; j < 4; ++j) F[2 + j] = *LDSP(bf16x8, hbase + (64 * wt + 16 * j) * 128);
  };
  auto rd = [&](bf16x8* F, int hbase, int cbase) {
#pragma unroll
    for (int i = 0; i < 2; ++i) F[i] = *LDSP(bf16x8, cbase + (32 * wd + 16 * i) * 128);
    rdh(F, hbase);
  };
  auto mm = [&](bf16x8* F) {
#pragma unroll
    for (int i = 0; i < 2; ++i)
#pragma unroll
      for (int j = 0; j < 4; ++j) acc[i][j] = __builtin_amdgcn_mfma_f32_16x16x32_bf16(F[i], F[2 + j], acc[i][j], 0, 0, 0);
  };
#pragma unroll
  for (int r = 4; r < 7; ++r)
#pragma unroll
    for (int i = 0; i < 2; ++i) {
      CR[r][0][i] = *LDSP(bf16x8, 40960 + (r - 4) * 8192 + a0 + (32 * wd + 16 * i) * 128);
      CR[r][1][i] = *LDSP(bf16x8, 40960 + (r - 4) * 8192 + a1 + (32 * wd + 16 * i) * 128);
    }
  rd(F0, 65536 + a0, cslot(0) + a0);
  SB0();
  rd(F1, 65536 + a1, cslot(0) + a1); mm(F0); ILV(6, 8)
  rd(F0, 81920 + a0, cslot(1) + a0); mm(F1); ILV(6, 8)
  rd(F1, 81920 + a1, cslot(1) + a1); mm(F0); ILV(6, 8)
#pragma unroll
  for (int r = 4; r < 7; ++r) asm volatile("" : "+v"(CR[r][0][0]), "+v"(CR[r][0][1]), "+v"(CR[r][1][0]), "+v"(CR[r][1][1]));
  WAIT_L0();
  BAR();
  int hi_ = 0, ci_ = 2;
#pragma unroll 1
  for (int s = 2; s < 25; ++s) {
    int hb = hi_ * 16384, cb = ci_ < 4 ? 131072 + ci_ * 8192 : 98304 + (ci_ - 4) * 8192;
    BAR();
    SB0();
    rd(F0, hb + a0, cb + a0);
    mm(F1);
    ILV(6, 8)
    rd(F1, hb + a1, cb + a1);
    mm(F0);
    ILV(6, 8)
    WAIT_L0();
    hi_ = hi_ == 5 ? 0 : hi_ + 1; ci_ = (ci_ + 1) & 7;
  }
#pragma unroll
  for (int r = 0; r < 7; ++r) {
    const int hb = ((25 + r + 4) % 6) * 16384;
    BAR();
    SB0();
    rdh(F0, hb + a0);
    mm(F1);
    ILV(4, 8)
    F0[0] = CR[r][0][0]; F0[1] = CR[r][0][1];
    rdh(F1, hb + a1);
    mm(F0);
    ILV(4, 8)
    F1[0] = CR[r][1][0]; F1[1] = CR[r][1][1];
    WAIT_L0();
  }
  mm(F1);
  MFMA_DRAIN();
  STAMP(9);
  const __amdgpu_buffer_rsrc_t yr = __builtin_amdgcn_make_buffer_rsrc((void*)y, 0, 2048 * 1024 * 4, 0x00020000);
#pragma unroll
  for (int dm = 0; dm < 2; ++dm) {
    int d = 64 * n + 32 * wd + 16 * dm + 4 * fq;
    float dk[4] = {__uint_as_float(ds[dm][0] << 16), __uint_as_float(ds[dm][0] & 0xffff0000u),
                   __uint_as_float(ds[dm][1] << 16), __uint_as_float(ds[dm][1] & 0xffff0000u)};
#pragma unroll
    for (int tn = 0; tn < 4; ++tn) {
      float xv[4] = {__uint_as_float(xs[dm][tn][0] << 16), __uint_as_float(xs[dm][tn][0] & 0xffff0000u),
                     __uint_as_float(xs[dm][tn][1] << 16), __uint_as_float(xs[dm][tn][1] & 0xffff0000u)};
      f32x4 o;
#pragma unroll
      for (int j = 0; j < 4; ++j) o[j] = 2.0f * acc[dm][tn][j] + bfround(xv[j] * dk[j]);
      __builtin_amdgcn_raw_buffer_store_b128(__builtin_bit_cast(u32x4, o), yr, ((128 * c + 64 * wt + 16 * tn + fr) * D + d) * 4, 0, 16);
    }
  }
  return;
  }
  {
    const int lw = wave - 4;
    int offC[2];
#pragma unroll
    for (int i = 0; i < 2; ++i) {
      int rho = 16 * lw + 8 * i + (lane >> 3);
      int chunk = (lane & 7) ^ ((rho >> 1) & 7);
      offC[i] = (64 * n + rho) * P + chunk * 8;
    }
    auto issueC = [&](int s, int slot_off) {
      const u16* Cb = ((s & 1) ? Cim : Cre) + coff(s);
#pragma unroll
      for (int i = 0; i < 2; ++i) GLDS(Cb + offC[i], slot_off + (2 * lw + i) * 1024);
    };
    {
      const u16* src[8];
#pragma unroll
      for (int i = 0; i < 4; ++i) {
        int rho = 32 * lw + 8 * i + (lane >> 3);
        int chunk = (lane & 7) ^ ((rho >> 1) & 7);
        int m = rho >> 4, i16 = rho & 15;
        int tau = 32 * (i16 >> 2) + 4 * m + (i16 & 3);
        src[i] = x + (size_t)(128 * c + tau) * D + chunk * 8;
        int p = 64 * g + 16 * (rho >> 5) + (rho & 15);
        src[4 + i] = ((rho & 16) ? Bim : Bre) + (size_t)p * D + chunk * 8;
      }
      auto issue = [&](int s0) {
        int buf = (s0 & 3) * ST1;
        int s = (s0 + koff) & 15;
#pragma unroll
        for (int i = 0; i < 4; ++i) GLDS(src[i] + s * 64, buf + (4 * lw + i) * 1024);
#pragma unroll
        for (int i = 0; i < 4; ++i) GLDS(src[4 + i] + s * 64, buf + 16384 + (4 * lw + i) * 1024);
      };
      issue(0); issue(1); issue(2);
      for (int s = 0; s < 6; ++s) { WAIT_V(16); BAR(); issue(s + 3); }
      WAIT_V(16); BAR(); issue(9);
      {
        const char* p1 = (const char*)Cre + b * 8192 + lw * 2048 + lane * 16;
        const char* p2 = (const char*)Cim + b * 8192 + lw * 2048 + lane * 16;
        GLDS(p1, 131072 + lw * 1024); GLDS(p1 + 1024, 131072 + lw * 1024);
        GLDS(p2, 131072 + lw * 1024); GLDS(p2 + 1024, 131072 + lw * 1024);
      }
      WAIT_V(20); BAR(); issue(10);
      WAIT_V(20); BAR(); issue(11);
      WAIT_V(20); BAR(); issue(12);
      WAIT_V(16); BAR(); issue(13);
      WAIT_V(16); BAR(); issue(14);
      WAIT_V(16); BAR(); issue(15);
      issueC(25, cslot(0)); issueC(26, cslot(1)); issueC(27, cslot(2)); issueC(28, cslot(3));
      WAIT_V(24); BAR();
      WAIT_V(16); BAR();
      WAIT_V(0); BAR();
    }
    const u16* srcH[4];
#pragma unroll
    for (int i = 0; i < 4; ++i) {
      int rho = 32 * lw + 8 * i + (lane >> 3);
      int chunk = (lane & 7) ^ ((rho >> 1) & 7);
      srcH[i] = h + (size_t)(128 * c + rho) * 2048 + chunk * 8;
    }
    auto issueH = [&](int s, int bi) {
      const int ho = coff(s) * 2 + (s & 1) * 64;
#pragma unroll
      for (int i = 0; i < 4; ++i) GLDS_SC1(srcH[i] + ho, bi * 16384 + (4 * lw + i) * 1024);
    };
    issueC(29, 40960); issueC(30, 49152); issueC(31, 57344);
    BAR();
    issueC(4, cslot(4)); issueC(5, cslot(5)); issueC(6, cslot(6)); issueC(7, cslot(7));
    BAR();
    issueC(0, cslot(0)); issueC(1, cslot(1)); issueC(2, cslot(2)); issueC(3, cslot(3));
    WAIT_V(0);
    BAR();
    if (lw == 0) {
      for (unsigned spin = 0;; ++spin) {
        u64 v = __hip_atomic_load(&flags[(c * 16 + (lane >> 2)) * 16 + (lane & 3)], __ATOMIC_RELAXED, __HIP_MEMORY_SCOPE_AGENT);
        if (__all(v == tag64) || spin > (1u << 22)) break;
        __builtin_amdgcn_s_sleep(2);
      }
    }
    BAR();
    issueH(2, 0);
    issueH(3, 1);
    issueH(4, 2);
    issueH(5, 3);
    WAIT_V(12); BAR();
    issueH(6, 4);
    issueC(8, cslot(0));
    issueH(7, 5);
    issueC(9, cslot(1));
    WAIT_V(20); BAR();
    issueH(8, 0);
    issueC(10, cslot(2));
    WAIT_V(22); BAR();
    issueH(9, 1);
    issueC(11, cslot(3));
    WAIT_V(24); BAR();
    issueH(10, 2);
    issueC(12, cslot(4));
    WAIT_V(26); BAR();
    issueH(11, 3);
    issueC(13, cslot(5));
    WAIT_V(26); BAR();
    issueH(12, 4);
    issueC(14, cslot(6));
    WAIT_V(26); BAR();
    issueH(13, 5);
    issueC(15, cslot(7));
    WAIT_V(26); BAR();
    issueH(14, 0);
    issueC(16, cslot(0));
    WAIT_V(26); BAR();
    issueH(15, 1);
    issueC(17, cslot(1));
    WAIT_V(26); BAR();
    issueH(16, 2);
    issueC(18, cslot(2));
    WAIT_V(26); BAR();
    issueH(17, 3);
    issueC(19, cslot(3));
    WAIT_V(26); BAR();
    issueH(18, 4);
    issueC(20, cslot(4));
    WAIT_V(26); BAR();
    issueH(19, 5);
    issueC(21, cslot(5));
    WAIT_V(26); BAR();
    issueH(20, 0);
    issueC(22, cslot(6));
    WAIT_V(26); BAR();
    issueH(21, 1);
    issueC(23, cslot(7));
    WAIT_V(26); BAR();
    issueH(22, 2);
    issueC(24, cslot(0));
    WAIT_V(26); BAR();
    issueH(23, 3);
    WAIT_V(24); BAR();
    issueH(24, 4);
    WAIT_V(22); BAR();
    issueH(25, 5);
    WAIT_V(20); BAR();
    issueH(26, 0);
    WAIT_V(18); BAR();
    issueH(27, 1);
    WAIT_V(16); BAR();
    issueH(28, 2);
    WAIT_V(16); BAR();
    issueH(29, 3);
    WAIT_V(16); BAR();
    issueH(30, 4);
    WAIT_V(16); BAR();
    issueH(31, 5);
    WAIT_V(16); BAR();
    WAIT_V(12); BAR();
    WAIT_V(8); BAR();
    WAIT_V(4); BAR();
    WAIT_V(0); BAR();
    return;
  }
}

extern "C" void kernel_launch(void* const* d_in, const int* in_sizes, int n_in, void* d_out, int out_size,
                              void* d_ws, size_t ws_size, hipStream_t stream) {
  const u16* x = (const u16*)d_in[0];
  const u16* Lre = (const u16*)d_in[1];
  const u16* Lim = (const u16*)d_in[2];
  const u16* Bre = (const u16*)d_in[3];
  const u16* Bim = (const u16*)d_in[4];
  const u16* Cre = (const u16*)d_in[5];
  const u16* Cim = (const u16*)d_in[6];
  const u16* logdt = (const u16*)d_in[7];
  const u16* Dskip = (const u16*)d_in[8];
  float* out = (float*)d_out;
  char* ws = (char*)d_ws;
  u32x4* exch = (u32x4*)ws;
  u64* flags = (u64*)(ws + 262144);
  u16* h = (u16*)(ws + (1 << 20));
  (void)hipFuncSetAttribute((const void*)s5_fused, hipFuncAttributeMaxDynamicSharedMemorySize, LDS_BYTES);
  s5_fused<<<256, 512, LDS_BYTES, stream>>>(x, Bre, Bim, Cre, Cim, Lre, Lim, logdt, Dskip, h, exch, flags, out TSPASS);
}
```

```cpp
#include <hip/hip_runtime.h>
#include <hip/hip_bf16.h>
#include <stdint.h>

typedef __attribute__((ext_vector_type(8))) short bf16x8;
typedef __attribute__((ext_vector_type(4))) short s16x4;
typedef __attribute__((ext_vector_type(4))) float f32x4;
typedef __attribute__((ext_vector_type(2))) float f32x2;
typedef __attribute__((ext_vector_type(4))) unsigned u32x4;
typedef __attribute__((ext_vector_type(2))) unsigned u32x2;
typedef unsigned short u16;
typedef unsigned long long u64;
#define LDSP(T, off) ((__attribute__((address_space(3))) T*)(smem + (off)))


static constexpr int D = 1024, P = 1024;

__device__ __forceinline__ float bf2f(u16 v) { return __uint_as_float(((unsigned)v) << 16); }
__device__ __forceinline__ float bfround(float f) { return __bfloat162float(__float2bfloat16(f)); }
typedef __attribute__((ext_vector_type(2))) __bf16 bf16x2;
__device__ __forceinline__ unsigned pack2(float a, float b) {
  return __builtin_bit_cast(unsigned, __builtin_convertvector(f32x2{a, b}, bf16x2));
}
#define WAIT_V(n) asm volatile("s_waitcnt vmcnt(%0)" ::"i"(n) : "memory")
#define WAIT_L0() asm volatile("s_waitcnt lgkmcnt(0)" ::: "memory")
#define BAR() asm volatile("s_barrier" ::: "memory")
#define SB0() __builtin_amdgcn_sched_barrier(0)
#define MFMA_DRAIN() do { SB0(); asm volatile("s_nop 15\n\ts_nop 15"); SB0(); } while (0)
#define ILV(NR, NM)                                                        \
  _Pragma("unroll") for (int i_ = 0; i_ < NR; ++i_) {                       \
    __builtin_amdgcn_sched_group_barrier(0x8, 1, 0);                        \
    __builtin_amdgcn_sched_group_barrier(0x100, 1, 0);                      \
  }                                                                         \
  __builtin_amdgcn_sched_group_barrier(0x8, NM - NR, 0);                    \
  SB0();
#define GLDS(gp, lo) __builtin_amdgcn_global_load_lds((const unsigned*)(gp), LDSP(unsigned, (lo)), 16, 0, 0)
#define GLDS_SC1(gp, lo) __builtin_amdgcn_global_load_lds((const unsigned*)(gp), LDSP(unsigned, (lo)), 16, 0, 16)

extern "C" __device__ unsigned long long s5_dispatch_id() __asm("llvm.amdgcn.dispatch.id");
__device__ __forceinline__ u32x2 launch_tag() {
  u64 z = s5_dispatch_id() * 0x9E3779B97F4A7C15ull + ((u64)__builtin_amdgcn_dispatch_ptr() >> 6) * 0xD1B54A32D192ED03ull + 0x2545F4914F6CDD1Dull;
  z = (z ^ (z >> 30)) * 0xBF58476D1CE4E5B9ull;
  z = (z ^ (z >> 27)) * 0x94D049BB133111EBull;
  z ^= z >> 31;
  unsigned a = (unsigned)z, b = (unsigned)(z >> 32);
  a = (a & 0xFFFF00FFu) | ((~a & 0xFFu) << 8);
  b = (b & 0xFFFF00FFu) | ((~b & 0xFFu) << 8);
  return u32x2{a, b};
}

__device__ __forceinline__ void sincos_acc(float a, float& sn, float& cs) {
  const double x = (double)a;
  const double k = __builtin_rint(x * 0.63661977236758134308);
  double r = __builtin_fma(-k, 1.57079632679489655800e+00, x);
  r = __builtin_fma(-k, 6.12323399573676603587e-17, r);
  const double z = r * r;
  double ps = __builtin_fma(z, -2.50521083854417187751e-08, 2.75573192239858906526e-06);
  ps = __builtin_fma(z, ps, -1.98412698412698412698e-04);
  ps = __builtin_fma(z, ps, 8.33333333333333333333e-03);
  ps = __builtin_fma(z, ps, -1.66666666666666666667e-01);
  const double sr = __builtin_fma(r * z, ps, r);
  double pc = __builtin_fma(z, 2.08767569878680989792e-09, -2.75573192239858906526e-07);
  pc = __builtin_fma(z, pc, 2.48015873015873015873e-05);
  pc = __builtin_fma(z, pc, -1.38888888888888888889e-03);
  pc = __builtin_fma(z, pc, 4.16666666666666666667e-02);
  pc = __builtin_fma(z, pc, -0.5);
  const double cr = __builtin_fma(z, pc, 1.0);
  const int q = (int)k;
  const float fs = (float)sr, fc = (float)cr;
  const float s0 = (q & 1) ? fc : fs, c0 = (q & 1) ? fs : fc;
  sn = (q & 2) ? -s0 : s0;
  cs = ((q + 1) & 2) ? -c0 : c0;
  if (__builtin_expect(!(__builtin_fabsf(a) < 1.0e6f), 0)) sincosf(a, &sn, &cs);
}

struct cd { double r, i; };
__device__ __forceinline__ cd cmul(cd a, cd b) { return cd{a.r * b.r - a.i * b.i, a.r * b.i + a.i * b.r}; }
__device__ __forceinline__ f32x2 tof(cd a) { return f32x2{(float)a.r, (float)a.i}; }
__device__ __forceinline__ f32x2 cfma(f32x2 a, f32x2 b, f32x2 c) {
  f32x2 t = __builtin_elementwise_fma(f32x2{a.x, a.x}, b, c);
  return __builtin_elementwise_fma(f32x2{-a.y, a.y}, f32x2{b.y, b.x}, t);
}

static constexpr int S1 = 16, ST1 = 32768;
static constexpr int LDS_BYTES = 163840;
static constexpr int IMG_PITCH = 288;
__device__ __forceinline__ constexpr int cslot(int i) { return i < 4 ? 131072 + i * 8192 : 98304 + (i - 4) * 8192; }

__global__ __launch_bounds__(512, 2) void s5_fused(const u16* __restrict__ x, const u16* __restrict__ Bre,
                                                   const u16* __restrict__ Bim, const u16* __restrict__ Cre,
                                                   const u16* __restrict__ Cim, const u16* __restrict__ Lre,
                                                   const u16* __restrict__ Lim, const u16* __restrict__ logdt,
                                                   const u16* __restrict__ Dskip, u16* h, u32x4* exch,
                                                   u64* flags, float* __restrict__ y) {
  extern __shared__ __attribute__((aligned(16))) char smem[];
  const int b = blockIdx.x, xcd = b & 7, idx = b >> 3;
  const int c = 4 * (xcd & 3) + (idx & 3), g = 8 * (xcd >> 2) + (idx >> 2);
  const int n = g;
  const int wave = __builtin_amdgcn_readfirstlane(threadIdx.x >> 6), lane = threadIdx.x & 63;
  const int koff = 4 * (((idx >> 2) + idx) & 3);
  const u32x2 tag = launch_tag();
  const u64 tag64 = (u64)tag.x | ((u64)tag.y << 32);
  auto coff = [&](int s) { int gb = s < 2 ? g : (((s - 2) >> 1) < g ? ((s - 2) >> 1) : (s >> 1)); return gb * 64; };
  if (wave < 4) {
  const int w = wave, fr = lane & 15, fq = lane >> 4;
  const int c0 = fq ^ (fr >> 1);
  const int a0 = fr * 128 + c0 * 16, a1 = fr * 128 + (c0 ^ 4) * 16;
  bf16x8 CR[7][2][2];
  const int wd = wave & 1, wt = wave >> 1;
  u32x2 xs[2][4], ds[2];
  ds[0] = *(const u32x2*)(Dskip + 64 * n + 32 * wd + 4 * fq);
  ds[1] = *(const u32x2*)(Dskip + 64 * n + 32 * wd + 16 + 4 * fq);
  {
    const int p = 64 * g + 16 * w + fr;
    f32x2 lam, cf, l8, l16, l24, l32, lq, mu4, mue;
    {
      float lr = bf2f(Lre[p]), li = bf2f(Lim[p]);
      float dt = bfround(expf(bf2f(logdt[p])));
      float ar = dt * lr, ai = dt * li;
      float e = expf(ar), sn, cs;
      sincos_acc(ai, sn, cs);
      lam = f32x2{e * cs, e * sn};
      float nr = lam.x - 1.0f, ni = lam.y;
      float den = lr * lr + li * li;
      cf = f32x2{(nr * lr + ni * li) / den, (ni * lr - nr * li) / den};
      lam.y = -lam.y;
      cd a{(double)lam.x, (double)lam.y};
      a = cmul(a, a); a = cmul(a, a); a = cmul(a, a);
      cd a8 = a, a16 = cmul(a8, a8), a24 = cmul(a16, a8), a32 = cmul(a16, a16), a64 = cmul(a32, a32), a96 = cmul(a64, a32);
      cd mu = cmul(a64, a64);
      l8 = tof(a8); l16 = tof(a16); l24 = tof(a24); l32 = tof(a32);
      cd mu2 = cmul(mu, mu), mu3 = cmul(mu2, mu);
      mu4 = tof(cmul(mu2, mu2));
      f32x2 f64_ = tof(a64), f96 = tof(a96), m1 = tof(mu), m2 = tof(mu2), m3 = tof(mu3);
      asm volatile("" : "+v"(f64_), "+v"(f96), "+v"(m1), "+v"(m2), "+v"(m3), "+v"(l32));
      const f32x2 one = {1.f, 0.f};
      const f32x2 qa = (fq & 1) ? l32 : one, qb = (fq & 1) ? f96 : f64_;
      lq = (fq & 2) ? qb : qa;
      const int e2 = (c - 1 - fq) & 3;
      const f32x2 ea = (e2 & 1) ? m1 : one, eb = (e2 & 1) ? m3 : m2;
      mue = (e2 & 2) ? eb : ea;
    }
    asm volatile("" : "+v"(lam), "+v"(cf), "+v"(l8), "+v"(l16), "+v"(l24), "+v"(l32), "+v"(lq), "+v"(mu4), "+v"(mue), "+v"(ds[0]), "+v"(ds[1]));
    f32x4 acc[8][2];
#pragma unroll
    for (int m = 0; m < 8; ++m) { acc[m][0] = f32x4{0, 0, 0, 0}; acc[m][1] = f32x4{0, 0, 0, 0}; }
    bf16x8 F0[10], F1[10];
    auto rd = [&](bf16x8* F, int base) {
#pragma unroll
      for (int nn = 0; nn < 2; ++nn) F[8 + nn] = *LDSP(bf16x8, base + 16384 + 4096 * w + 2048 * nn);
#pragma unroll
      for (int m = 0; m < 8; ++m) F[m] = *LDSP(bf16x8, base + 2048 * m);
    };
    auto mm = [&](bf16x8* F) {
#pragma unroll
      for (int m = 0; m < 8; ++m)
#pragma unroll
        for (int nn = 0; nn < 2; ++nn) acc[m][nn] = __builtin_amdgcn_mfma_f32_16x16x32_bf16(F[m], F[8 + nn], acc[m][nn], 0, 0, 0);
    };
    BAR();
    rd(F0, a0);
    SB0();
    rd(F1, a1);
    mm(F0);
    ILV(10, 16)
    WAIT_L0();
    const int sx = (n - koff) & 15;
    int s = 1;
#pragma unroll 1
    for (int part = 0; part < 2; ++part) {
      const int hi = part ? S1 : sx + 1;
#pragma unroll 1
      for (; s < hi; ++s) {
      int buf = (s & 3) * ST1;
      BAR();
      SB0();
      rd(F0, buf + a0);
      mm(F1);
      ILV(10, 16)
      rd(F1, buf + a1);
      mm(F0);
      ILV(10, 16)
      WAIT_L0();
      }
      if (part == 0) {
#pragma unroll
        for (int tn = 0; tn < 4; ++tn) {
          const int tau = 64 * wt + 16 * tn + fr;
          const int rho = 16 * ((tau >> 2) & 7) + 4 * (tau >> 5) + (tau & 3);
#pragma unroll
          for (int dm = 0; dm < 2; ++dm)
            xs[dm][tn] = *LDSP(u32x2, (sx & 3) * ST1 + rho * 128 + (((4 * wd + 2 * dm + (fq >> 1)) ^ ((rho >> 1) & 7)) * 16) + (fq & 1) * 8);
        }
        WAIT_L0();
        SB0();
      }
    }
    BAR();
    mm(F1);
    MFMA_DRAIN();
    f32x2 u[32];
#pragma unroll
    for (int m = 0; m < 8; ++m)
#pragma unroll
      for (int j = 0; j < 4; ++j) {
        float a = acc[m][0][j], bb = acc[m][1][j];
        f32x2 t = f32x2{-cf.y, -cf.x} * f32x2{bb, bb};
        u[4 * m + j] = __builtin_elementwise_fma(f32x2{cf.x, -cf.y}, f32x2{a, a}, t);
      }
    f32x2 ag[4];
#pragma unroll
    for (int k = 0; k < 4; ++k) ag[k] = u[8 * k];
#pragma unroll
    for (int i = 1; i < 8; ++i)
#pragma unroll
      for (int k = 0; k < 4; ++k) ag[k] = cfma(lam, ag[k], u[8 * k + i]);
    f32x2 e1 = ag[0], e2 = cfma(l8, e1, ag[1]), e3 = cfma(l8, e2, ag[2]), la = cfma(l8, e3, ag[3]);
    f32x2 q[4];
#pragma unroll
    for (int k = 0; k < 4; ++k) q[k] = f32x2{__shfl(la.x, fr + 16 * k), __shfl(la.y, fr + 16 * k)};
    f32x2 E1 = q[0], E2 = cfma(l32, E1, q[1]), E3 = cfma(l32, E2, q[2]), AG = cfma(l32, E3, q[3]);
    const __amdgpu_buffer_rsrc_t er = __builtin_amdgcn_make_buffer_rsrc((void*)exch, 0, 16 * P * 16, 0x00020000);
    if (fq == 0 && c < 15)
      __builtin_amdgcn_raw_buffer_store_b128(u32x4{__float_as_uint(AG.x), tag.x, __float_as_uint(AG.y), tag.y}, er, (c * P + p) * 16, 0, 16);
    const f32x2 Ea = (fq & 1) ? E1 : f32x2{0.f, 0.f}, Eb = (fq & 1) ? E3 : E2;
    f32x2 E = (fq & 2) ? Eb : Ea;
    f32x2 car = {0.f, 0.f};
#pragma unroll
    for (int r = 0; r < 4; ++r) {
#pragma unroll
      for (int i = 0; i < 2; ++i) {
        CR[r][0][i] = *LDSP(bf16x8, cslot(r) + a0 + (32 * (w & 1) + 16 * i) * 128);
        CR[r][1][i] = *LDSP(bf16x8, cslot(r) + a1 + (32 * (w & 1) + 16 * i) * 128);
      }
      asm volatile("" : "+v"(CR[r][0][0]), "+v"(CR[r][0][1]), "+v"(CR[r][1][0]), "+v"(CR[r][1][1]));
    }
    if (c > 0) {
      u32x4 v[4];
      for (unsigned spin = 0;; ++spin) {
        bool ok = true;
#pragma unroll
        for (int i = 0; i < 4; ++i) {
          int j = min(fq + 4 * i, c - 1);
          v[i] = __builtin_amdgcn_raw_buffer_load_b128(er, (j * P + p) * 16, 0, 16);
          ok &= (v[i][1] == tag.x) & (v[i][3] == tag.y);
        }
        if (__all(ok) || spin > (1u << 20)) break;
        __builtin_amdgcn_s_sleep(1);
        asm volatile("" ::: "memory");
      }
#pragma unroll
      for (int i = 0; i < 4; ++i) {
        f32x2 vv = {__uint_as_float(v[i][0]), __uint_as_float(v[i][2])};
        if (fq + 4 * i < c) car = i == 0 ? vv : cfma(mu4, car, vv);
      }
      if (fq < c) car = cfma(mue, car, f32x2{0.f, 0.f});
      car.x += __shfl_xor(car.x, 16); car.y += __shfl_xor(car.y, 16);
      car.x += __shfl_xor(car.x, 32); car.y += __shfl_xor(car.y, 32);
    }
    f32x2 H = cfma(lq, car, E);
    f32x2 hs[4] = {H, cfma(l8, H, e1), cfma(l16, H, e2), cfma(l24, H, e3)};
#pragma unroll
    for (int i = 0; i < 8; ++i)
#pragma unroll
      for (int k = 0; k < 4; ++k) { hs[k] = cfma(lam, hs[k], u[8 * k + i]); u[8 * k + i] = hs[k]; }
#pragma unroll
    for (int i = 0; i < 4; ++i) {
      u32x4 vr, vi;
#pragma unroll
      for (int j = 0; j < 4; ++j) {
        vr[j] = pack2(u[8 * i + 2 * j].x, u[8 * i + 2 * j + 1].x);
        vi[j] = pack2(u[8 * i + 2 * j].y, u[8 * i + 2 * j + 1].y);
      }
      *LDSP(u32x4, (16 * w + fr) * IMG_PITCH + 64 * fq + 16 * i) = vr;
      *LDSP(u32x4, (64 + 16 * w + fr) * IMG_PITCH + 64 * fq + 16 * i) = vi;
    }
    WAIT_L0();
    BAR();
    {
      const __amdgpu_buffer_rsrc_t hr = __builtin_amdgcn_make_buffer_rsrc((void*)h, 0, 2048 * 2048 * 2, 0x00020000);
      const int a = (lane >> 2) & 3, bb = lane & 3;
      const int ib = (8 * fq + a) * IMG_PITCH + (32 * w + 4 * bb) * 2;
      bf16x8 o[8];
#pragma unroll
      for (int tb = 0; tb < 2; ++tb)
#pragma unroll
        for (int cq = 0; cq < 4; ++cq) {
          s16x4 lo = __builtin_amdgcn_ds_read_tr16_b64_v4i16(LDSP(s16x4, ib + (32 * cq) * IMG_PITCH + 32 * tb));
          s16x4 hi = __builtin_amdgcn_ds_read_tr16_b64_v4i16(LDSP(s16x4, ib + (32 * cq + 4) * IMG_PITCH + 32 * tb));
          o[4 * tb + cq] = __builtin_shufflevector(lo, hi, 0, 1, 2, 3, 4, 5, 6, 7);
        }
      SB0();
      const int ob = ((128 * c + 32 * w + fr) * 2048 + 128 * g + 8 * fq) * 2;
#pragma unroll
      for (int tb = 0; tb < 2; ++tb)
#pragma unroll
        for (int cq = 0; cq < 4; ++cq)
        {
          __builtin_amdgcn_raw_buffer_store_b128(__builtin_bit_cast(u32x4, o[4 * tb + cq]), hr, ob + tb * 65536 + cq * 64, 0, 16);
          *LDSP(bf16x8, 65536 + (cq >> 1) * 16384 + (32 * w + 16 * tb + fr) * 128 + (((4 * (cq & 1) + fq) ^ ((fr >> 1) & 7)) * 16)) = o[4 * tb + cq];
        }
    }
    WAIT_V(0);
    if (lane == 0) __hip_atomic_store(&flags[(c * 16 + g) * 16 + w], tag64, __ATOMIC_RELAXED, __HIP_MEMORY_SCOPE_AGENT);
    WAIT_L0();
    BAR();
  }
  f32x4 acc[2][4];
#pragma unroll
  for (int i = 0; i < 2; ++i)
#pragma unroll
    for (int j = 0; j < 4; ++j) acc[i][j] = f32x4{0, 0, 0, 0};
  bf16x8 F0[6], F1[6];
  auto rdh = [&](bf16x8* F, int hbase) {
#pragma unroll
    for (int j = 0; j < 4; ++j) F[2 + j] = *LDSP(bf16x8, hbase + (64 * wt + 16 * j) * 128);
  };
  auto rd = [&](bf16x8* F, int hbase, int cbase) {
#pragma unroll
    for (int i = 0; i < 2; ++i) F[i] = *LDSP(bf16x8, cbase + (32 * wd + 16 * i) * 128);
    rdh(F, hbase);
  };
  auto mm = [&](bf16x8* F) {
#pragma unroll
    for (int i = 0; i < 2; ++i)
#pragma unroll
      for (int j = 0; j < 4; ++j) acc[i][j] = __builtin_amdgcn_mfma_f32_16x16x32_bf16(F[i], F[2 + j], acc[i][j], 0, 0, 0);
  };
  int hl0 = a0 + 64 * wt * 128, hl1 = a1 + 64 * wt * 128, hh0 = hl0 + 65536, hh1 = hl1 + 65536;
  int cc0 = a0 + 32 * wd * 128 + 131072, cc1 = a1 + 32 * wd * 128 + 131072;
  asm volatile("" : "+v"(hl0), "+v"(hl1), "+v"(hh0), "+v"(hh1), "+v"(cc0), "+v"(cc1));
  auto rdh2 = [&](bf16x8* F, int base) {
#pragma unroll
    for (int j = 0; j < 4; ++j) F[2 + j] = *LDSP(bf16x8, base + 2048 * j);
  };
  auto rdo = [&](bf16x8* F, int hbase, int cbase) {
    F[0] = *LDSP(bf16x8, cbase); F[1] = *LDSP(bf16x8, cbase + 2048);
    rdh2(F, hbase);
  };
#pragma unroll
  for (int r = 4; r < 7; ++r)
#pragma unroll
    for (int i = 0; i < 2; ++i) {
      CR[r][0][i] = *LDSP(bf16x8, 40960 + (r - 4) * 8192 + a0 + (32 * wd + 16 * i) * 128);
      CR[r][1][i] = *LDSP(bf16x8, 40960 + (r - 4) * 8192 + a1 + (32 * wd + 16 * i) * 128);
    }
  rdo(F0, hh0, cc0);
  SB0();
  rdo(F1, hh1, cc1); mm(F0); ILV(6, 8)
  rdo(F0, hh0 + 16384, cc0 + 8192); mm(F1); ILV(6, 8)
  rdo(F1, hh1 + 16384, cc1 + 8192); mm(F0); ILV(6, 8)
  MFMA_DRAIN();
#pragma unroll
  for (int r = 4; r < 7; ++r) asm volatile("" : "+v"(CR[r][0][0]), "+v"(CR[r][0][1]), "+v"(CR[r][1][0]), "+v"(CR[r][1][1]));
  WAIT_L0();
  BAR();
  if (wave == 0) {
    for (unsigned spin = 0;; ++spin) {
      u64 v = __hip_atomic_load(&flags[(c * 16 + (lane >> 2)) * 16 + (lane & 3)], __ATOMIC_RELAXED, __HIP_MEMORY_SCOPE_AGENT);
      if (__all(v == tag64) || spin > (1u << 20)) break;
      __builtin_amdgcn_s_sleep(2);
    }
  }
  int hi_ = 0, ci_ = 2;
#pragma unroll 1
  for (int s = 2; s < 25; ++s) {
    int hb = hi_ * 16384, cb = ci_ < 4 ? 131072 + ci_ * 8192 : 98304 + (ci_ - 4) * 8192;
    BAR();
    SB0();
    rd(F0, hb + a0, cb + a0);
    mm(F1);
    ILV(6, 8)
    rd(F1, hb + a1, cb + a1);
    mm(F0);
    ILV(6, 8)
    WAIT_L0();
    hi_ = hi_ == 5 ? 0 : hi_ + 1; ci_ = (ci_ + 1) & 7;
  }
#pragma unroll
  for (int r = 0; r < 7; ++r) {
    const int sl = (25 + r + 4) % 6;
    BAR();
    SB0();
    rdh2(F0, sl < 4 ? hl0 + sl * 16384 : hh0 + (sl - 4) * 16384);
    mm(F1);
    ILV(4, 8)
    F0[0] = CR[r][0][0]; F0[1] = CR[r][0][1];
    rdh2(F1, sl < 4 ? hl1 + sl * 16384 : hh1 + (sl - 4) * 16384);
    mm(F0);
    ILV(4, 8)
    F1[0] = CR[r][1][0]; F1[1] = CR[r][1][1];
    WAIT_L0();
  }
  mm(F1);
  MFMA_DRAIN();
  const __amdgpu_buffer_rsrc_t yr = __builtin_amdgcn_make_buffer_rsrc((void*)y, 0, 2048 * 1024 * 4, 0x00020000);
#pragma unroll
  for (int dm = 0; dm < 2; ++dm) {
    int d = 64 * n + 32 * wd + 16 * dm + 4 * fq;
    float dk[4] = {__uint_as_float(ds[dm][0] << 16), __uint_as_float(ds[dm][0] & 0xffff0000u),
                   __uint_as_float(ds[dm][1] << 16), __uint_as_float(ds[dm][1] & 0xffff0000u)};
#pragma unroll
    for (int tn = 0; tn < 4; ++tn) {
      float xv[4] = {__uint_as_float(xs[dm][tn][0] << 16), __uint_as_float(xs[dm][tn][0] & 0xffff0000u),
                     __uint_as_float(xs[dm][tn][1] << 16), __uint_as_float(xs[dm][tn][1] & 0xffff0000u)};
      f32x4 o;
#pragma unroll
      for (int j = 0; j < 4; ++j) o[j] = 2.0f * acc[dm][tn][j] + bfround(xv[j] * dk[j]);
      __builtin_amdgcn_raw_buffer_store_b128(__builtin_bit_cast(u32x4, o), yr, ((128 * c + 64 * wt + 16 * tn + fr) * D + d) * 4, 0, 16);
    }
  }
  return;
  }
  {
    const int lw = wave - 4;
    int offC[2];
#pragma unroll
    for (int i = 0; i < 2; ++i) {
      int rho = 16 * lw + 8 * i + (lane >> 3);
      int chunk = (lane & 7) ^ ((rho >> 1) & 7);
      offC[i] = (64 * n + rho) * P + chunk * 8;
    }
    auto issueC = [&](int s, int slot_off) {
      const u16* Cb = ((s & 1) ? Cim : Cre) + coff(s);
#pragma unroll
      for (int i = 0; i < 2; ++i) GLDS(Cb + offC[i], slot_off + (2 * lw + i) * 1024);
    };
    {
      const u16* src[8];
#pragma unroll
      for (int i = 0; i < 4; ++i) {
        int rho = 32 * lw + 8 * i + (lane >> 3);
        int chunk = (lane & 7) ^ ((rho >> 1) & 7);
        int m = rho >> 4, i16 = rho & 15;
        int tau = 32 * (i16 >> 2) + 4 * m + (i16 & 3);
        src[i] = x + (size_t)(128 * c + tau) * D + chunk * 8;
        int p = 64 * g + 16 * (rho >> 5) + (rho & 15);
        src[4 + i] = ((rho & 16) ? Bim : Bre) + (size_t)p * D + chunk * 8;
      }
      auto issue = [&](int s0) {
        int buf = (s0 & 3) * ST1;
        int s = (s0 + koff) & 15;
#pragma unroll
        for (int i = 0; i < 4; ++i) GLDS(src[i] + s * 64, buf + (4 * lw + i) * 1024);
#pragma unroll
        for (int i = 0; i < 4; ++i) GLDS(src[4 + i] + s * 64, buf + 16384 + (4 * lw + i) * 1024);
      };
      issue(0); issue(1); issue(2);
#pragma unroll 1
      for (int s = 0; s < 6; ++s) { WAIT_V(16); BAR(); issue(s + 3); }
      WAIT_V(16); BAR(); issue(9);
      {
        const char* p1 = (const char*)Cre + b * 8192 + lw * 2048 + lane * 16;
        const char* p2 = (const char*)Cim + b * 8192 + lw * 2048 + lane * 16;
        GLDS(p1, 131072 + lw * 1024); GLDS(p1 + 1024, 131072 + lw * 1024);
        GLDS(p2, 131072 + lw * 1024); GLDS(p2 + 1024, 131072 + lw * 1024);
      }
      WAIT_V(20); BAR(); issue(10);
      WAIT_V(20); BAR(); issue(11);
      WAIT_V(20); BAR(); issue(12);
      WAIT_V(16); BAR(); issue(13);
      WAIT_V(16); BAR(); issue(14);
      WAIT_V(16); BAR(); issue(15);
      issueC(25, cslot(0)); issueC(26, cslot(1)); issueC(27, cslot(2)); issueC(28, cslot(3));
      WAIT_V(24); BAR();
      WAIT_V(16); BAR();
      WAIT_V(0); BAR();
    }
    const u16* srcH[4];
#pragma unroll
    for (int i = 0; i < 4; ++i) {
      int rho = 32 * lw + 8 * i + (lane >> 3);
      int chunk = (lane & 7) ^ ((rho >> 1) & 7);
      srcH[i] = h + (size_t)(128 * c + rho) * 2048 + chunk * 8;
    }
    auto issueH = [&](int s, int bi) {
      const int ho = coff(s) * 2 + (s & 1) * 64;
#pragma unroll
      for (int i = 0; i < 4; ++i) GLDS_SC1(srcH[i] + ho, bi * 16384 + (4 * lw + i) * 1024);
    };
    issueC(29, 40960); issueC(30, 49152); issueC(31, 57344);
    BAR();
    issueC(4, cslot(4)); issueC(5, cslot(5)); issueC(6, cslot(6)); issueC(7, cslot(7));
    BAR();
    issueC(0, cslot(0)); issueC(1, cslot(1)); issueC(2, cslot(2)); issueC(3, cslot(3));
    WAIT_V(0);
    BAR();
    if (lw == 0) {
      const bool first = (lane >> 2) * 64 == coff(2) || (lane >> 2) * 64 == coff(4);
      for (unsigned spin = 0;; ++spin) {
        u64 v = __hip_atomic_load(&flags[(c * 16 + (lane >> 2)) * 16 + (lane & 3)], __ATOMIC_RELAXED, __HIP_MEMORY_SCOPE_AGENT);
        if (__all(v == tag64 || !first) || spin > (1u << 20)) break;
        __builtin_amdgcn_s_sleep(2);
      }
    }
    BAR();
    issueH(2, 0);
    issueH(3, 1);
    issueH(4, 2);
    issueH(5, 3);
    WAIT_V(12); BAR();
    issueH(6, 4);
    issueC(8, cslot(0));
    issueH(7, 5);
    issueC(9, cslot(1));
    WAIT_V(20); BAR();
    issueH(8, 0);
    issueC(10, cslot(2));
    WAIT_V(22); BAR();
    issueH(9, 1);
    issueC(11, cslot(3));
    WAIT_V(24); BAR();
    issueH(10, 2);
    issueC(12, cslot(4));
    {
      int hb = 3, cb = 5;
#pragma unroll 1
      for (int st = 6; st < 18; ++st) {
        WAIT_V(26); BAR();
        issueH(st + 5, hb);
        issueC(st + 7, cb < 4 ? 131072 + cb * 8192 : 98304 + (cb - 4) * 8192);
        hb = hb == 5 ? 0 : hb + 1; cb = (cb + 1) & 7;
      }
    }
    WAIT_V(26); BAR();
    issueH(23, 3);
    WAIT_V(24); BAR();
    issueH(24, 4);
    WAIT_V(22); BAR();
    issueH(25, 5);
    WAIT_V(20); BAR();
    issueH(26, 0);
    WAIT_V(18); BAR();
    issueH(27, 1);
    WAIT_V(16); BAR();
    issueH(28, 2);
    WAIT_V(16); BAR();
    issueH(29, 3);
    WAIT_V(16); BAR();
    issueH(30, 4);
    WAIT_V(16); BAR();
    issueH(31, 5);
    WAIT_V(16); BAR();
    WAIT_V(12); BAR();
    WAIT_V(8); BAR();
    WAIT_V(4); BAR();
    WAIT_V(0); BAR();
    return;
  }
}

extern "C" void kernel_launch(void* const* d_in, const int* in_sizes, int n_in, void* d_out, int out_size,
                              void* d_ws, size_t ws_size, hipStream_t stream) {
  const u16* x = (const u16*)d_in[0];
  const u16* Lre = (const u16*)d_in[1];
  const u16* Lim = (const u16*)d_in[2];
  const u16* Bre = (const u16*)d_in[3];
  const u16* Bim = (const u16*)d_in[4];
  const u16* Cre = (const u16*)d_in[5];
  const u16* Cim = (const u16*)d_in[6];
  const u16* logdt = (const u16*)d_in[7];
  const u16* Dskip = (const u16*)d_in[8];
  float* out = (float*)d_out;
  char* ws = (char*)d_ws;
  u32x4* exch = (u32x4*)ws;
  u64* flags = (u64*)(ws + 262144);
  u16* h = (u16*)(ws + (1 << 20));
  (void)hipFuncSetAttribute((const void*)s5_fused, hipFuncAttributeMaxDynamicSharedMemorySize, LDS_BYTES);
  s5_fused<<<256, 512, LDS_BYTES, stream>>>(x, Bre, Bim, Cre, Cim, Lre, Lim, logdt, Dskip, h, exch, flags, out);
}
```

```cpp
#include <hip/hip_runtime.h>
#include <hip/hip_bf16.h>
#include <stdint.h>

typedef __attribute__((ext_vector_type(8))) short bf16x8;
typedef __attribute__((ext_vector_type(4))) short s16x4;
typedef __attribute__((ext_vector_type(4))) float f32x4;
typedef __attribute__((ext_vector_type(2))) float f32x2;
typedef __attribute__((ext_vector_type(4))) unsigned u32x4;
typedef __attribute__((ext_vector_type(2))) unsigned u32x2;
typedef unsigned short u16;
typedef unsigned long long u64;
#define LDSP(T, off) ((__attribute__((address_space(3))) T*)(smem + (off)))


static constexpr int D = 1024, P = 1024;

__device__ __forceinline__ float bf2f(u16 v) { return __uint_as_float(((unsigned)v) << 16); }
__device__ __forceinline__ float bfround(float f) { return __bfloat162float(__float2bfloat16(f)); }
typedef __attribute__((ext_vector_type(2))) __bf16 bf16x2;
__device__ __forceinline__ unsigned pack2(float a, float b) {
  return __builtin_bit_cast(unsigned, __builtin_convertvector(f32x2{a, b}, bf16x2));
}
#define WAIT_V(n) asm volatile("s_waitcnt vmcnt(%0)" ::"i"(n) : "memory")
#define WAIT_L0() asm volatile("s_waitcnt lgkmcnt(0)" ::: "memory")
#define BAR() asm volatile("s_barrier" ::: "memory")
#define SB0() __builtin_amdgcn_sched_barrier(0)
#define MFMA_DRAIN() do { SB0(); asm volatile("s_nop 15\n\ts_nop 15"); SB0(); } while (0)
#define ILV(NR, NM)                                                        \
  _Pragma("unroll") for (int i_ = 0; i_ < NR; ++i_) {                       \
    __builtin_amdgcn_sched_group_barrier(0x8, 1, 0);                        \
    __builtin_amdgcn_sched_group_barrier(0x100, 1, 0);                      \
  }                                                                         \
  __builtin_amdgcn_sched_group_barrier(0x8, NM - NR, 0);                    \
  SB0();
#define GLDS(gp, lo) __builtin_amdgcn_global_load_lds((const unsigned*)(gp), LDSP(unsigned, (lo)), 16, 0, 0)
#define GLDS_SC1(gp, lo) __builtin_amdgcn_global_load_lds((const unsigned*)(gp), LDSP(unsigned, (lo)), 16, 0, 16)

extern "C" __device__ unsigned long long s5_dispatch_id() __asm("llvm.amdgcn.dispatch.id");
__device__ __forceinline__ u32x2 launch_tag() {
  u64 z = s5_dispatch_id() * 0x9E3779B97F4A7C15ull + ((u64)__builtin_amdgcn_dispatch_ptr() >> 6) * 0xD1B54A32D192ED03ull + 0x2545F4914F6CDD1Dull;
  z = (z ^ (z >> 30)) * 0xBF58476D1CE4E5B9ull;
  z = (z ^ (z >> 27)) * 0x94D049BB133111EBull;
  z ^= z >> 31;
  unsigned a = (unsigned)z, b = (unsigned)(z >> 32);
  a = (a & 0xFFFF00FFu) | ((~a & 0xFFu) << 8);
  b = (b & 0xFFFF00FFu) | ((~b & 0xFFu) << 8);
  return u32x2{a, b};
}

__device__ __forceinline__ void sincos_acc(float a, float& sn, float& cs) {
  const double x = (double)a;
  const double k = __builtin_rint(x * 0.63661977236758134308);
  double r = __builtin_fma(-k, 1.57079632679489655800e+00, x);
  r = __builtin_fma(-k, 6.12323399573676603587e-17, r);
  const double z = r * r;
  double ps = __builtin_fma(z, -2.50521083854417187751e-08, 2.75573192239858906526e-06);
  ps = __builtin_fma(z, ps, -1.98412698412698412698e-04);
  ps = __builtin_fma(z, ps, 8.33333333333333333333e-03);
  ps = __builtin_fma(z, ps, -1.66666666666666666667e-01);
  const double sr = __builtin_fma(r * z, ps, r);
  double pc = __builtin_fma(z, 2.08767569878680989792e-09, -2.75573192239858906526e-07);
  pc = __builtin_fma(z, pc, 2.48015873015873015873e-05);
  pc = __builtin_fma(z, pc, -1.38888888888888888889e-03);
  pc = __builtin_fma(z, pc, 4.16666666666666666667e-02);
  pc = __builtin_fma(z, pc, -0.5);
  const double cr = __builtin_fma(z, pc, 1.0);
  const int q = (int)k;
  const float fs = (float)sr, fc = (float)cr;
  const float s0 = (q & 1) ? fc : fs, c0 = (q & 1) ? fs : fc;
  sn = (q & 2) ? -s0 : s0;
  cs = ((q + 1) & 2) ? -c0 : c0;
  if (__builtin_expect(!(__builtin_fabsf(a) < 1.0e6f), 0)) sincosf(a, &sn, &cs);
}

struct cd { double r, i; };
__device__ __forceinline__ cd cmul(cd a, cd b) { return cd{a.r * b.r - a.i * b.i, a.r * b.i + a.i * b.r}; }
__device__ __forceinline__ f32x2 tof(cd a) { return f32x2{(float)a.r, (float)a.i}; }
__device__ __forceinline__ f32x2 cfma(f32x2 a, f32x2 b, f32x2 c) {
  f32x2 t = __builtin_elementwise_fma(f32x2{a.x, a.x}, b, c);
  return __builtin_elementwise_fma(f32x2{-a.y, a.y}, f32x2{b.y, b.x}, t);
}

static constexpr int S1 = 16, ST1 = 32768;
static constexpr int LDS_BYTES = 163840;
static constexpr int IMG_PITCH = 288;
__device__ __forceinline__ constexpr int cslot(int i) { return i < 4 ? 131072 + i * 8192 : 98304 + (i - 4) * 8192; }

__global__ __launch_bounds__(512, 2) void s5_fused(const u16* __restrict__ x, const u16* __restrict__ Bre,
                                                   const u16* __restrict__ Bim, const u16* __restrict__ Cre,
                                                   const u16* __restrict__ Cim, const u16* __restrict__ Lre,
                                                   const u16* __restrict__ Lim, const u16* __restrict__ logdt,
                                                   const u16* __restrict__ Dskip, u16* h, u32x4* exch,
                                                   u64* flags, float* __restrict__ y) {
  extern __shared__ __attribute__((aligned(16))) char smem[];
  const int b = blockIdx.x, xcd = b & 7, idx = b >> 3;
  const int c = 4 * (xcd & 3) + (idx & 3), g = 8 * (xcd >> 2) + (idx >> 2);
  const int n = g;
  const int wave = __builtin_amdgcn_readfirstlane(threadIdx.x >> 6), lane = threadIdx.x & 63;
  const int koff = 4 * (((idx >> 2) + idx) & 3);
  const u32x2 tag = launch_tag();
  const u64 tag64 = (u64)tag.x | ((u64)tag.y << 32);
  auto coff = [&](int s) { int gb = s < 2 ? g : (((s - 2) >> 1) < g ? ((s - 2) >> 1) : (s >> 1)); return gb * 64; };
  if (wave < 4) {
  const int w = wave, fr = lane & 15, fq = lane >> 4;
  const int c0 = fq ^ (fr >> 1);
  const int a0 = fr * 128 + c0 * 16, a1 = fr * 128 + (c0 ^ 4) * 16;
  bf16x8 CR[7][2][2];
  const int wd = wave & 1, wt = wave >> 1;
  u32x2 xs[2][4], ds[2];
  ds[0] = *(const u32x2*)(Dskip + 64 * n + 32 * wd + 4 * fq);
  ds[1] = *(const u32x2*)(Dskip + 64 * n + 32 * wd + 16 + 4 * fq);
  {
    const int p = 64 * g + 16 * w + fr;
    f32x2 lam, cf, l8, l16, l24, l32, lq, mu4, mue;
    {
      float lr = bf2f(Lre[p]), li = bf2f(Lim[p]);
      float dt = bfround(expf(bf2f(logdt[p])));
      float ar = dt * lr, ai = dt * li;
      float e = expf(ar), sn, cs;
      sincos_acc(ai, sn, cs);
      lam = f32x2{e * cs, e * sn};
      float nr = lam.x - 1.0f, ni = lam.y;
      float den = lr * lr + li * li;
      cf = f32x2{(nr * lr + ni * li) / den, (ni * lr - nr * li) / den};
      lam.y = -lam.y;
      cd a{(double)lam.x, (double)lam.y};
      a = cmul(a, a); a = cmul(a, a); a = cmul(a, a);
      cd a8 = a, a16 = cmul(a8, a8), a24 = cmul(a16, a8), a32 = cmul(a16, a16), a64 = cmul(a32, a32), a96 = cmul(a64, a32);
      cd mu = cmul(a64, a64);
      l8 = tof(a8); l16 = tof(a16); l24 = tof(a24); l32 = tof(a32);
      cd mu2 = cmul(mu, mu), mu3 = cmul(mu2, mu);
      mu4 = tof(cmul(mu2, mu2));
      f32x2 f64_ = tof(a64), f96 = tof(a96), m1 = tof(mu), m2 = tof(mu2), m3 = tof(mu3);
      asm volatile("" : "+v"(f64_), "+v"(f96), "+v"(m1), "+v"(m2), "+v"(m3), "+v"(l32));
      const f32x2 one = {1.f, 0.f};
      const f32x2 qa = (fq & 1) ? l32 : one, qb = (fq & 1) ? f96 : f64_;
      lq = (fq & 2) ? qb : qa;
      const int e2 = (c - 1 - fq) & 3;
      const f32x2 ea = (e2 & 1) ? m1 : one, eb = (e2 & 1) ? m3 : m2;
      mue = (e2 & 2) ? eb : ea;
    }
    asm volatile("" : "+v"(lam), "+v"(cf), "+v"(l8), "+v"(l16), "+v"(l24), "+v"(l32), "+v"(lq), "+v"(mu4), "+v"(mue), "+v"(ds[0]), "+v"(ds[1]));
    f32x4 acc[8][2];
#pragma unroll
    for (int m = 0; m < 8; ++m) { acc[m][0] = f32x4{0, 0, 0, 0}; acc[m][1] = f32x4{0, 0, 0, 0}; }
    bf16x8 F0[10], F1[10];
    auto rd = [&](bf16x8* F, int base) {
#pragma unroll
      for (int nn = 0; nn < 2; ++nn) F[8 + nn] = *LDSP(bf16x8, base + 16384 + 4096 * w + 2048 * nn);
#pragma unroll
      for (int m = 0; m < 8; ++m) F[m] = *LDSP(bf16x8, base + 2048 * m);
    };
    auto mm = [&](bf16x8* F) {
#pragma unroll
      for (int m = 0; m < 8; ++m)
#pragma unroll
        for (int nn = 0; nn < 2; ++nn) acc[m][nn] = __builtin_amdgcn_mfma_f32_16x16x32_bf16(F[m], F[8 + nn], acc[m][nn], 0, 0, 0);
    };
    BAR();
    rd(F0, a0);
    SB0();
    rd(F1, a1);
    mm(F0);
    ILV(10, 16)
    WAIT_L0();
    const int sx = (n - koff) & 15;
    int s = 1;
#pragma unroll 1
    for (int part = 0; part < 2; ++part) {
      const int hi = part ? S1 : sx + 1;
#pragma unroll 1
      for (; s < hi; ++s) {
      int buf = (s & 3) * ST1;
      BAR();
      SB0();
      rd(F0, buf + a0);
      mm(F1);
      ILV(10, 16)
      rd(F1, buf + a1);
      mm(F0);
      ILV(10, 16)
      WAIT_L0();
      }
      if (part == 0) {
#pragma unroll
        for (int tn = 0; tn < 4; ++tn) {
          const int tau = 64 * wt + 16 * tn + fr;
          const int rho = 16 * ((tau >> 2) & 7) + 4 * (tau >> 5) + (tau & 3);
#pragma unroll
          for (int dm = 0; dm < 2; ++dm)
            xs[dm][tn] = *LDSP(u32x2, (sx & 3) * ST1 + rho * 128 + (((4 * wd + 2 * dm + (fq >> 1)) ^ ((rho >> 1) & 7)) * 16) + (fq & 1) * 8);
        }
        WAIT_L0();
        SB0();
      }
    }
    BAR();
    mm(F1);
    MFMA_DRAIN();
    f32x2 u[32];
#pragma unroll
    for (int m = 0; m < 8; ++m)
#pragma unroll
      for (int j = 0; j < 4; ++j) {
        float a = acc[m][0][j], bb = acc[m][1][j];
        f32x2 t = f32x2{-cf.y, -cf.x} * f32x2{bb, bb};
        u[4 * m + j] = __builtin_elementwise_fma(f32x2{cf.x, -cf.y}, f32x2{a, a}, t);
      }
    f32x2 ag[4];
#pragma unroll
    for (int k = 0; k < 4; ++k) ag[k] = u[8 * k];
#pragma unroll
    for (int i = 1; i < 8; ++i)
#pragma unroll
      for (int k = 0; k < 4; ++k) ag[k] = cfma(lam, ag[k], u[8 * k + i]);
    f32x2 e1 = ag[0], e2 = cfma(l8, e1, ag[1]), e3 = cfma(l8, e2, ag[2]), la = cfma(l8, e3, ag[3]);
    f32x2 q[4];
#pragma unroll
    for (int k = 0; k < 4; ++k) q[k] = f32x2{__shfl(la.x, fr + 16 * k), __shfl(la.y, fr + 16 * k)};
    f32x2 E1 = q[0], E2 = cfma(l32, E1, q[1]), E3 = cfma(l32, E2, q[2]), AG = cfma(l32, E3, q[3]);
    const __amdgpu_buffer_rsrc_t er = __builtin_amdgcn_make_buffer_rsrc((void*)exch, 0, 16 * P * 16, 0x00020000);
    if (fq == 0 && c < 15)
      __builtin_amdgcn_raw_buffer_store_b128(u32x4{__float_as_uint(AG.x), tag.x, __float_as_uint(AG.y), tag.y}, er, (c * P + p) * 16, 0, 16);
    const f32x2 Ea = (fq & 1) ? E1 : f32x2{0.f, 0.f}, Eb = (fq & 1) ? E3 : E2;
    f32x2 E = (fq & 2) ? Eb : Ea;
    f32x2 car = {0.f, 0.f};
#pragma unroll
    for (int r = 0; r < 4; ++r) {
#pragma unroll
      for (int i = 0; i < 2; ++i) {
        CR[r][0][i] = *LDSP(bf16x8, cslot(r) + a0 + (32 * (w & 1) + 16 * i) * 128);
        CR[r][1][i] = *LDSP(bf16x8, cslot(r) + a1 + (32 * (w & 1) + 16 * i) * 128);
      }
      asm volatile("" : "+v"(CR[r][0][0]), "+v"(CR[r][0][1]), "+v"(CR[r][1][0]), "+v"(CR[r][1][1]));
    }
    if (c > 0) {
      u32x4 v[4];
      for (unsigned spin = 0;; ++spin) {
        bool ok = true;
#pragma unroll
        for (int i = 0; i < 4; ++i) {
          int j = min(fq + 4 * i, c - 1);
          v[i] = __builtin_amdgcn_raw_buffer_load_b128(er, (j * P + p) * 16, 0, 16);
          ok &= (v[i][1] == tag.x) & (v[i][3] == tag.y);
        }
        if (__all(ok) || spin > (1u << 20)) break;
        __builtin_amdgcn_s_sleep(1);
        asm volatile("" ::: "memory");
      }
#pragma unroll
      for (int i = 0; i < 4; ++i) {
        f32x2 vv = {__uint_as_float(v[i][0]), __uint_as_float(v[i][2])};
        if (fq + 4 * i < c) car = i == 0 ? vv : cfma(mu4, car, vv);
      }
      if (fq < c) car = cfma(mue, car, f32x2{0.f, 0.f});
      car.x += __shfl_xor(car.x, 16); car.y += __shfl_xor(car.y, 16);
      car.x += __shfl_xor(car.x, 32); car.y += __shfl_xor(car.y, 32);
    }
    f32x2 H = cfma(lq, car, E);
    f32x2 hs[4] = {H, cfma(l8, H, e1), cfma(l16, H, e2), cfma(l24, H, e3)};
#pragma unroll
    for (int i = 0; i < 8; ++i)
#pragma unroll
      for (int k = 0; k < 4; ++k) { hs[k] = cfma(lam, hs[k], u[8 * k + i]); u[8 * k + i] = hs[k]; }
#pragma unroll
    for (int i = 0; i < 4; ++i) {
      u32x4 vr, vi;
#pragma unroll
      for (int j = 0; j < 4; ++j) {
        vr[j] = pack2(u[8 * i + 2 * j].x, u[8 * i + 2 * j + 1].x);
        vi[j] = pack2(u[8 * i + 2 * j].y, u[8 * i + 2 * j + 1].y);
      }
      *LDSP(u32x4, (16 * w + fr) * IMG_PITCH + 64 * fq + 16 * i) = vr;
      *LDSP(u32x4, (64 + 16 * w + fr) * IMG_PITCH + 64 * fq + 16 * i) = vi;
    }
    WAIT_L0();
    BAR();
    {
      const __amdgpu_buffer_rsrc_t hr = __builtin_amdgcn_make_buffer_rsrc((void*)h, 0, 2048 * 2048 * 2, 0x00020000);
      const int a = (lane >> 2) & 3, bb = lane & 3;
      const int ib = (8 * fq + a) * IMG_PITCH + (32 * w + 4 * bb) * 2;
      bf16x8 o[8];
#pragma unroll
      for (int tb = 0; tb < 2; ++tb)
#pragma unroll
        for (int cq = 0; cq < 4; ++cq) {
          s16x4 lo = __builtin_amdgcn_ds_read_tr16_b64_v4i16(LDSP(s16x4, ib + (32 * cq) * IMG_PITCH + 32 * tb));
          s16x4 hi = __builtin_amdgcn_ds_read_tr16_b64_v4i16(LDSP(s16x4, ib + (32 * cq + 4) * IMG_PITCH + 32 * tb));
          o[4 * tb + cq] = __builtin_shufflevector(lo, hi, 0, 1, 2, 3, 4, 5, 6, 7);
        }
      SB0();
      const int ob = ((128 * c + 32 * w + fr) * 2048 + 128 * g + 8 * fq) * 2;
#pragma unroll
      for (int tb = 0; tb < 2; ++tb)
#pragma unroll
        for (int cq = 0; cq < 4; ++cq)
        {
          __builtin_amdgcn_raw_buffer_store_b128(__builtin_bit_cast(u32x4, o[4 * tb + cq]), hr, ob + tb * 65536 + cq * 64, 0, 16);
          *LDSP(bf16x8, 65536 + (cq >> 1) * 16384 + (32 * w + 16 * tb + fr) * 128 + (((4 * (cq & 1) + fq) ^ ((fr >> 1) & 7)) * 16)) = o[4 * tb + cq];
        }
    }
    WAIT_V(0);
    if (lane == 0) __hip_atomic_store(&flags[(c * 16 + g) * 16 + w], tag64, __ATOMIC_RELAXED, __HIP_MEMORY_SCOPE_AGENT);
    WAIT_L0();
    BAR();
  }
  f32x4 acc[2][4];
#pragma unroll
  for (int i = 0; i < 2; ++i)
#pragma unroll
    for (int j = 0; j < 4; ++j) acc[i][j] = f32x4{0, 0, 0, 0};
  bf16x8 F0[6], F1[6];
  auto rdh = [&](bf16x8* F, int hbase) {
#pragma unroll
    for (int j = 0; j < 4; ++j) F[2 + j] = *LDSP(bf16x8, hbase + (64 * wt + 16 * j) * 128);
  };
  auto rd = [&](bf16x8* F, int hbase, int cbase) {
#pragma unroll
    for (int i = 0; i < 2; ++i) F[i] = *LDSP(bf16x8, cbase + (32 * wd + 16 * i) * 128);
    rdh(F, hbase);
  };
  auto mm = [&](bf16x8* F) {
#pragma unroll
    for (int i = 0; i < 2; ++i)
#pragma unroll
      for (int j = 0; j < 4; ++j) acc[i][j] = __builtin_amdgcn_mfma_f32_16x16x32_bf16(F[i], F[2 + j], acc[i][j], 0, 0, 0);
  };
  int hl0 = a0 + 64 * wt * 128, hl1 = a1 + 64 * wt * 128, hh0 = hl0 + 65536, hh1 = hl1 + 65536;
  int cc0 = a0 + 32 * wd * 128 + 131072, cc1 = a1 + 32 * wd * 128 + 131072;
  asm volatile("" : "+v"(hl0), "+v"(hl1), "+v"(hh0), "+v"(hh1), "+v"(cc0), "+v"(cc1));
  auto rdh2 = [&](bf16x8* F, int base) {
#pragma unroll
    for (int j = 0; j < 4; ++j) F[2 + j] = *LDSP(bf16x8, base + 2048 * j);
  };
  auto rdo = [&](bf16x8* F, int hbase, int cbase) {
    F[0] = *LDSP(bf16x8, cbase); F[1] = *LDSP(bf16x8, cbase + 2048);
    rdh2(F, hbase);
  };
#pragma unroll
  for (int r = 4; r < 7; ++r)
#pragma unroll
    for (int i = 0; i < 2; ++i) {
      CR[r][0][i] = *LDSP(bf16x8, 40960 + (r - 4) * 8192 + a0 + (32 * wd + 16 * i) * 128);
      CR[r][1][i] = *LDSP(bf16x8, 40960 + (r - 4) * 8192 + a1 + (32 * wd + 16 * i) * 128);
    }
  rdo(F0, hh0, cc0);
  SB0();
  rdo(F1, hh1, cc1); mm(F0); ILV(6, 8)
  rdo(F0, hh0 + 16384, cc0 + 8192); mm(F1); ILV(6, 8)
  rdo(F1, hh1 + 16384, cc1 + 8192); mm(F0); ILV(6, 8)
  MFMA_DRAIN();
#pragma unroll
  for (int r = 4; r < 7; ++r) asm volatile("" : "+v"(CR[r][0][0]), "+v"(CR[r][0][1]), "+v"(CR[r][1][0]), "+v"(CR[r][1][1]));
  WAIT_L0();
  BAR();
  if (wave == 0) {
    for (unsigned spin = 0;; ++spin) {
      u64 v = __hip_atomic_load(&flags[(c * 16 + (lane >> 2)) * 16 + (lane & 3)], __ATOMIC_RELAXED, __HIP_MEMORY_SCOPE_AGENT);
      if (__all(v == tag64) || spin > (1u << 20)) break;
      __builtin_amdgcn_s_sleep(2);
    }
  }
  int hi_ = 0, ci_ = 2;
#pragma unroll 1
  for (int s = 2; s < 25; ++s) {
    int hb = hi_ * 16384, cb = ci_ < 4 ? 131072 + ci_ * 8192 : 98304 + (ci_ - 4) * 8192;
    BAR();
    SB0();
    rd(F0, hb + a0, cb + a0);
    mm(F1);
    ILV(6, 8)
    rd(F1, hb + a1, cb + a1);
    mm(F0);
    ILV(6, 8)
    WAIT_L0();
    hi_ = hi_ == 5 ? 0 : hi_ + 1; ci_ = (ci_ + 1) & 7;
  }
#pragma unroll
  for (int r = 0; r < 7; ++r) {
    const int sl = (25 + r + 4) % 6;
    BAR();
    SB0();
    rdh2(F0, sl < 4 ? hl0 + sl * 16384 : hh0 + (sl - 4) * 16384);
    mm(F1);
    ILV(4, 8)
    F0[0] = CR[r][0][0]; F0[1] = CR[r][0][1];
    rdh2(F1, sl < 4 ? hl1 + sl * 16384 : hh1 + (sl - 4) * 16384);
    mm(F0);
    ILV(4, 8)
    F1[0] = CR[r][1][0]; F1[1] = CR[r][1][1];
    WAIT_L0();
  }
  mm(F1);
  MFMA_DRAIN();
  const __amdgpu_buffer_rsrc_t yr = __builtin_amdgcn_make_buffer_rsrc((void*)y, 0, 2048 * 1024 * 4, 0x00020000);
#pragma unroll
  for (int dm = 0; dm < 2; ++dm) {
    int d = 64 * n + 32 * wd + 16 * dm + 4 * fq;
    float dk[4] = {__uint_as_float(ds[dm][0] << 16), __uint_as_float(ds[dm][0] & 0xffff0000u),
                   __uint_as_float(ds[dm][1] << 16), __uint_as_float(ds[dm][1] & 0xffff0000u)};
#pragma unroll
    for (int tn = 0; tn < 4; ++tn) {
      float xv[4] = {__uint_as_float(xs[dm][tn][0] << 16), __uint_as_float(xs[dm][tn][0] & 0xffff0000u),
                     __uint_as_float(xs[dm][tn][1] << 16), __uint_as_float(xs[dm][tn][1] & 0xffff0000u)};
      f32x4 o;
#pragma unroll
      for (int j = 0; j < 4; ++j) o[j] = 2.0f * acc[dm][tn][j] + bfround(xv[j] * dk[j]);
      __builtin_amdgcn_raw_buffer_store_b128(__builtin_bit_cast(u32x4, o), yr, ((128 * c + 64 * wt + 16 * tn + fr) * D + d) * 4, 0, 16);
    }
  }
  return;
  }
  {
    const int lw = wave - 4;
    int offC[2];
#pragma unroll
    for (int i = 0; i < 2; ++i) {
      int rho = 16 * lw + 8 * i + (lane >> 3);
      int chunk = (lane & 7) ^ ((rho >> 1) & 7);
      offC[i] = (64 * n + rho) * P + chunk * 8;
    }
    auto issueC = [&](int s, int slot_off) {
      const u16* Cb = ((s & 1) ? Cim : Cre) + coff(s);
#pragma unroll
      for (int i = 0; i < 2; ++i) GLDS(Cb + offC[i], slot_off + (2 * lw + i) * 1024);
    };
    {
      const u16* src[8];
#pragma unroll
      for (int i = 0; i < 4; ++i) {
        int rho = 32 * lw + 8 * i + (lane >> 3);
        int chunk = (lane & 7) ^ ((rho >> 1) & 7);
        int m = rho >> 4, i16 = rho & 15;
        int tau = 32 * (i16 >> 2) + 4 * m + (i16 & 3);
        src[i] = x + (size_t)(128 * c + tau) * D + chunk * 8;
        int p = 64 * g + 16 * (rho >> 5) + (rho & 15);
        src[4 + i] = ((rho & 16) ? Bim : Bre) + (size_t)p * D + chunk * 8;
      }
      auto issue = [&](int s0) {
        int buf = (s0 & 3) * ST1;
        int s = (s0 + koff) & 15;
#pragma unroll
        for (int i = 0; i < 4; ++i) GLDS(src[i] + s * 64, buf + (4 * lw + i) * 1024);
#pragma unroll
        for (int i = 0; i < 4; ++i) GLDS(src[4 + i] + s * 64, buf + 16384 + (4 * lw + i) * 1024);
      };
#pragma unroll 1
      for (int s = 0; s < 3; ++s) issue(s);
#pragma unroll 1
      for (int s = 0; s < 13; ++s) {
        WAIT_V(16); BAR(); issue(s + 3);
        if (s == 6) {
          const char* p1 = (const char*)Cre + b * 8192 + lw * 2048 + lane * 16;
          const char* p2 = (const char*)Cim + b * 8192 + lw * 2048 + lane * 16;
          GLDS(p1, 131072 + lw * 1024); GLDS(p1 + 1024, 131072 + lw * 1024);
          GLDS(p2, 131072 + lw * 1024); GLDS(p2 + 1024, 131072 + lw * 1024);
        }
      }
      issueC(25, cslot(0)); issueC(26, cslot(1)); issueC(27, cslot(2)); issueC(28, cslot(3));
      WAIT_V(24); BAR();
      WAIT_V(16); BAR();
      WAIT_V(0); BAR();
    }
    const u16* srcH[4];
#pragma unroll
    for (int i = 0; i < 4; ++i) {
      int rho = 32 * lw + 8 * i + (lane >> 3);
      int chunk = (lane & 7) ^ ((rho >> 1) & 7);
      srcH[i] = h + (size_t)(128 * c + rho) * 2048 + chunk * 8;
    }
    auto issueH = [&](int s, int bi) {
      const int ho = coff(s) * 2 + (s & 1) * 64;
#pragma unroll
      for (int i = 0; i < 4; ++i) GLDS_SC1(srcH[i] + ho, bi * 16384 + (4 * lw + i) * 1024);
    };
    issueC(29, 40960); issueC(30, 49152); issueC(31, 57344);
    BAR();
    issueC(4, cslot(4)); issueC(5, cslot(5)); issueC(6, cslot(6)); issueC(7, cslot(7));
    BAR();
    issueC(0, cslot(0)); issueC(1, cslot(1)); issueC(2, cslot(2)); issueC(3, cslot(3));
    WAIT_V(0);
    BAR();
    if (lw == 0) {
      const bool first = (lane >> 2) * 64 == coff(2) || (lane >> 2) * 64 == coff(4);
      for (unsigned spin = 0;; ++spin) {
        u64 v = __hip_atomic_load(&flags[(c * 16 + (lane >> 2)) * 16 + (lane & 3)], __ATOMIC_RELAXED, __HIP_MEMORY_SCOPE_AGENT);
        if (__all(v == tag64 || !first) || spin > (1u << 20)) break;
        __builtin_amdgcn_s_sleep(2);
      }
    }
    BAR();
    issueH(2, 0);
    issueH(3, 1);
    issueH(4, 2);
    issueH(5, 3);
    WAIT_V(12); BAR();
    issueH(6, 4);
    issueC(8, cslot(0));
    issueH(7, 5);
    issueC(9, cslot(1));
    WAIT_V(20); BAR();
    issueH(8, 0);
    issueC(10, cslot(2));
    WAIT_V(22); BAR();
    issueH(9, 1);
    issueC(11, cslot(3));
    WAIT_V(24); BAR();
    issueH(10, 2);
    issueC(12, cslot(4));
    {
      int hb = 3, cb = 5;
#pragma unroll 1
      for (int st = 6; st < 18; ++st) {
        WAIT_V(26); BAR();
        issueH(st + 5, hb);
        issueC(st + 7, cb < 4 ? 131072 + cb * 8192 : 98304 + (cb - 4) * 8192);
        hb = hb == 5 ? 0 : hb + 1; cb = (cb + 1) & 7;
      }
#pragma unroll 1
      for (int st = 18; st < 27; ++st) {
        WAIT_V(16); BAR();
        issueH(st + 5, hb);
        hb = hb == 5 ? 0 : hb + 1;
      }
    }
    WAIT_V(16); BAR();
    WAIT_V(12); BAR();
    WAIT_V(8); BAR();
    WAIT_V(4); BAR();
    WAIT_V(0); BAR();
    return;
  }
}

extern "C" void kernel_launch(void* const* d_in, const int* in_sizes, int n_in, void* d_out, int out_size,
                              void* d_ws, size_t ws_size, hipStream_t stream) {
  const u16* x = (const u16*)d_in[0];
  const u16* Lre = (const u16*)d_in[1];
  const u16* Lim = (const u16*)d_in[2];
  const u16* Bre = (const u16*)d_in[3];
  const u16* Bim = (const u16*)d_in[4];
  const u16* Cre = (const u16*)d_in[5];
  const u16* Cim = (const u16*)d_in[6];
  const u16* logdt = (const u16*)d_in[7];
  const u16* Dskip = (const u16*)d_in[8];
  float* out = (float*)d_out;
  char* ws = (char*)d_ws;
  u32x4* exch = (u32x4*)ws;
  u64* flags = (u64*)(ws + 262144);
  u16* h = (u16*)(ws + (1 << 20));
  (void)hipFuncSetAttribute((const void*)s5_fused, hipFuncAttributeMaxDynamicSharedMemorySize, LDS_BYTES);
  s5_fused<<<256, 512, LDS_BYTES, stream>>>(x, Bre, Bim, Cre, Cim, Lre, Lim, logdt, Dskip, h, exch, flags, out);
}
```

```cpp
#include <hip/hip_runtime.h>
#include <hip/hip_bf16.h>
#include <stdint.h>

typedef __attribute__((ext_vector_type(8))) short bf16x8;
typedef __attribute__((ext_vector_type(4))) short s16x4;
typedef __attribute__((ext_vector_type(4))) float f32x4;
typedef __attribute__((ext_vector_type(2))) float f32x2;
typedef __attribute__((ext_vector_type(4))) unsigned u32x4;
typedef __attribute__((ext_vector_type(2))) unsigned u32x2;
typedef unsigned short u16;
typedef unsigned long long u64;
#define LDSP(T, off) ((__attribute__((address_space(3))) T*)(smem + (off)))


static constexpr int D = 1024, P = 1024;

__device__ __forceinline__ float bf2f(u16 v) { return __uint_as_float(((unsigned)v) << 16); }
__device__ __forceinline__ float bfround(float f) { return __bfloat162float(__float2bfloat16(f)); }
typedef __attribute__((ext_vector_type(2))) __bf16 bf16x2;
__device__ __forceinline__ unsigned pack2(float a, float b) {
  return __builtin_bit_cast(unsigned, __builtin_convertvector(f32x2{a, b}, bf16x2));
}
#define WAIT_V(n) asm volatile("s_waitcnt vmcnt(%0)" ::"i"(n) : "memory")
#define WAIT_L0() asm volatile("s_waitcnt lgkmcnt(0)" ::: "memory")
#define BAR() asm volatile("s_barrier" ::: "memory")
#define SB0() __builtin_amdgcn_sched_barrier(0)
#define MFMA_DRAIN() do { SB0(); asm volatile("s_nop 15\n\ts_nop 15"); SB0(); } while (0)
#define ILV(NR, NM)                                                        \
  _Pragma("unroll") for (int i_ = 0; i_ < NR; ++i_) {                       \
    __builtin_amdgcn_sched_group_barrier(0x8, 1, 0);                        \
    __builtin_amdgcn_sched_group_barrier(0x100, 1, 0);                      \
  }                                                                         \
  __builtin_amdgcn_sched_group_barrier(0x8, NM - NR, 0);                    \
  SB0();
#define GLDS(gp, lo) __builtin_amdgcn_global_load_lds((const unsigned*)(gp), LDSP(unsigned, (lo)), 16, 0, 0)
#define GLDS_SC1(gp, lo) __builtin_amdgcn_global_load_lds((const unsigned*)(gp), LDSP(unsigned, (lo)), 16, 0, 16)

extern "C" __device__ unsigned long long s5_dispatch_id() __asm("llvm.amdgcn.dispatch.id");
__device__ __forceinline__ u32x2 launch_tag() {
  u64 z = s5_dispatch_id() * 0x9E3779B97F4A7C15ull + ((u64)__builtin_amdgcn_dispatch_ptr() >> 6) * 0xD1B54A32D192ED03ull + 0x2545F4914F6CDD1Dull;
  z = (z ^ (z >> 30)) * 0xBF58476D1CE4E5B9ull;
  z = (z ^ (z >> 27)) * 0x94D049BB133111EBull;
  z ^= z >> 31;
  unsigned a = (unsigned)z, b = (unsigned)(z >> 32);
  a = (a & 0xFFFF00FFu) | ((~a & 0xFFu) << 8);
  b = (b & 0xFFFF00FFu) | ((~b & 0xFFu) << 8);
  return u32x2{a, b};
}

__device__ __forceinline__ void sincos_acc(float a, float& sn, float& cs) {
  const double x = (double)a;
  const double k = __builtin_rint(x * 0.63661977236758134308);
  double r = __builtin_fma(-k, 1.57079632679489655800e+00, x);
  r = __builtin_fma(-k, 6.12323399573676603587e-17, r);
  const double z = r * r;
  double ps = __builtin_fma(z, -2.50521083854417187751e-08, 2.75573192239858906526e-06);
  ps = __builtin_fma(z, ps, -1.98412698412698412698e-04);
  ps = __builtin_fma(z, ps, 8.33333333333333333333e-03);
  ps = __builtin_fma(z, ps, -1.66666666666666666667e-01);
  const double sr = __builtin_fma(r * z, ps, r);
  double pc = __builtin_fma(z, 2.08767569878680989792e-09, -2.75573192239858906526e-07);
  pc = __builtin_fma(z, pc, 2.48015873015873015873e-05);
  pc = __builtin_fma(z, pc, -1.38888888888888888889e-03);
  pc = __builtin_fma(z, pc, 4.16666666666666666667e-02);
  pc = __builtin_fma(z, pc, -0.5);
  const double cr = __builtin_fma(z, pc, 1.0);
  const int q = (int)k;
  const float fs = (float)sr, fc = (float)cr;
  const float s0 = (q & 1) ? fc : fs, c0 = (q & 1) ? fs : fc;
  sn = (q & 2) ? -s0 : s0;
  cs = ((q + 1) & 2) ? -c0 : c0;
  if (__builtin_expect(!(__builtin_fabsf(a) < 1.0e6f), 0)) sincosf(a, &sn, &cs);
}

struct cd { double r, i; };
__device__ __forceinline__ cd cmul(cd a, cd b) { return cd{a.r * b.r - a.i * b.i, a.r * b.i + a.i * b.r}; }
__device__ __forceinline__ f32x2 tof(cd a) { return f32x2{(float)a.r, (float)a.i}; }
__device__ __forceinline__ f32x2 cfma(f32x2 a, f32x2 b, f32x2 c) {
  f32x2 t = __builtin_elementwise_fma(f32x2{a.x, a.x}, b, c);
  return __builtin_elementwise_fma(f32x2{-a.y, a.y}, f32x2{b.y, b.x}, t);
}

static constexpr int S1 = 16, ST1 = 32768;
static constexpr int LDS_BYTES = 163840;
static constexpr int IMG_PITCH = 288;
__device__ __forceinline__ constexpr int cslot(int i) { return i < 4 ? 131072 + i * 8192 : 98304 + (i - 4) * 8192; }

__global__ __launch_bounds__(512, 2) void s5_fused(const u16* __restrict__ x, const u16* __restrict__ Bre,
                                                   const u16* __restrict__ Bim, const u16* __restrict__ Cre,
                                                   const u16* __restrict__ Cim, const u16* __restrict__ Lre,
                                                   const u16* __restrict__ Lim, const u16* __restrict__ logdt,
                                                   const u16* __restrict__ Dskip, u16* h, u32x4* exch,
                                                   u64* flags, float* __restrict__ y) {
  extern __shared__ __attribute__((aligned(16))) char smem[];
  const int b = blockIdx.x, xcd = b & 7, idx = b >> 3;
  const int c = 4 * (xcd & 3) + (idx & 3), g = 8 * (xcd >> 2) + (idx >> 2);
  const int n = g;
  const int wave = __builtin_amdgcn_readfirstlane(threadIdx.x >> 6), lane = threadIdx.x & 63;
  const int koff = 4 * (((idx >> 2) + idx) & 3);
  const u32x2 tag = launch_tag();
  const u64 tag64 = (u64)tag.x | ((u64)tag.y << 32);
  auto coff = [&](int s) { int gb = s < 2 ? g : (((s - 2) >> 1) < g ? ((s - 2) >> 1) : (s >> 1)); return gb * 64; };
  if (wave < 4) {
  const int w = wave, fr = lane & 15, fq = lane >> 4;
  const int c0 = fq ^ (fr >> 1);
  const int a0 = fr * 128 + c0 * 16, a1 = fr * 128 + (c0 ^ 4) * 16;
  bf16x8 CR[7][2][2];
  const int wd = wave & 1, wt = wave >> 1;
  u32x2 xs[2][4], ds[2];
  ds[0] = *(const u32x2*)(Dskip + 64 * n + 32 * wd + 4 * fq);
  ds[1] = *(const u32x2*)(Dskip + 64 * n + 32 * wd + 16 + 4 * fq);
  {
    const int p = 64 * g + 16 * w + fr;
    f32x2 lam, cf, l8, l16, l24, l32, lq, mu4, mue;
    {
      float lr = bf2f(Lre[p]), li = bf2f(Lim[p]);
      float dt = bfround(expf(bf2f(logdt[p])));
      float ar = dt * lr, ai = dt * li;
      float e = expf(ar), sn, cs;
      sincos_acc(ai, sn, cs);
      lam = f32x2{e * cs, e * sn};
      float nr = lam.x - 1.0f, ni = lam.y;
      float den = lr * lr + li * li;
      cf = f32x2{(nr * lr + ni * li) / den, (ni * lr - nr * li) / den};
      lam.y = -lam.y;
      cd a{(double)lam.x, (double)lam.y};
      a = cmul(a, a); a = cmul(a, a); a = cmul(a, a);
      cd a8 = a, a16 = cmul(a8, a8), a24 = cmul(a16, a8), a32 = cmul(a16, a16), a64 = cmul(a32, a32), a96 = cmul(a64, a32);
      cd mu = cmul(a64, a64);
      l8 = tof(a8); l16 = tof(a16); l24 = tof(a24); l32 = tof(a32);
      cd mu2 = cmul(mu, mu), mu3 = cmul(mu2, mu);
      mu4 = tof(cmul(mu2, mu2));
      f32x2 f64_ = tof(a64), f96 = tof(a96), m1 = tof(mu), m2 = tof(mu2), m3 = tof(mu3);
      asm volatile("" : "+v"(f64_), "+v"(f96), "+v"(m1), "+v"(m2), "+v"(m3), "+v"(l32));
      const f32x2 one = {1.f, 0.f};
      const f32x2 qa = (fq & 1) ? l32 : one, qb = (fq & 1) ? f96 : f64_;
      lq = (fq & 2) ? qb : qa;
      const int e2 = (c - 1 - fq) & 3;
      const f32x2 ea = (e2 & 1) ? m1 : one, eb = (e2 & 1) ? m3 : m2;
      mue = (e2 & 2) ? eb : ea;
    }
    asm volatile("" : "+v"(lam), "+v"(cf), "+v"(l8), "+v"(l16), "+v"(l24), "+v"(l32), "+v"(lq), "+v"(mu4), "+v"(mue), "+v"(ds[0]), "+v"(ds[1]));
    f32x4 acc[8][2];
#pragma unroll
    for (int m = 0; m < 8; ++m) { acc[m][0] = f32x4{0, 0, 0, 0}; acc[m][1] = f32x4{0, 0, 0, 0}; }
    bf16x8 F0[10], F1[10];
    auto rd = [&](bf16x8* F, int base) {
#pragma unroll
      for (int nn = 0; nn < 2; ++nn) F[8 + nn] = *LDSP(bf16x8, base + 16384 + 4096 * w + 2048 * nn);
#pragma unroll
      for (int m = 0; m < 8; ++m) F[m] = *LDSP(bf16x8, base + 2048 * m);
    };
    auto mm = [&](bf16x8* F) {
#pragma unroll
      for (int m = 0; m < 8; ++m)
#pragma unroll
        for (int nn = 0; nn < 2; ++nn) acc[m][nn] = __builtin_amdgcn_mfma_f32_16x16x32_bf16(F[m], F[8 + nn], acc[m][nn], 0, 0, 0);
    };
    BAR();
    rd(F0, a0);
    SB0();
    rd(F1, a1);
    mm(F0);
    ILV(10, 16)
    WAIT_L0();
    const int sx = (n - koff) & 15;
    int s = 1;
#pragma unroll 1
    for (int part = 0; part < 2; ++part) {
      const int hi = part ? S1 : sx + 1;
#pragma unroll 1
      for (; s < hi; ++s) {
      int buf = (s & 3) * ST1;
      BAR();
      SB0();
      rd(F0, buf + a0);
      mm(F1);
      ILV(10, 16)
      rd(F1, buf + a1);
      mm(F0);
      ILV(10, 16)
      WAIT_L0();
      }
      if (part == 0) {
#pragma unroll
        for (int tn = 0; tn < 4; ++tn) {
          const int tau = 64 * wt + 16 * tn + fr;
          const int rho = 16 * ((tau >> 2) & 7) + 4 * (tau >> 5) + (tau & 3);
#pragma unroll
          for (int dm = 0; dm < 2; ++dm)
            xs[dm][tn] = *LDSP(u32x2, (sx & 3) * ST1 + rho * 128 + (((4 * wd + 2 * dm + (fq >> 1)) ^ ((rho >> 1) & 7)) * 16) + (fq & 1) * 8);
        }
        WAIT_L0();
        SB0();
      }
    }
    BAR();
    mm(F1);
    MFMA_DRAIN();
    f32x2 u[32];
#pragma unroll
    for (int m = 0; m < 8; ++m)
#pragma unroll
      for (int j = 0; j < 4; ++j) {
        float a = acc[m][0][j], bb = acc[m][1][j];
        f32x2 t = f32x2{-cf.y, -cf.x} * f32x2{bb, bb};
        u[4 * m + j] = __builtin_elementwise_fma(f32x2{cf.x, -cf.y}, f32x2{a, a}, t);
      }
    f32x2 ag[4];
#pragma unroll
    for (int k = 0; k < 4; ++k) ag[k] = u[8 * k];
#pragma unroll
    for (int i = 1; i < 8; ++i)
#pragma unroll
      for (int k = 0; k < 4; ++k) ag[k] = cfma(lam, ag[k], u[8 * k + i]);
    f32x2 e1 = ag[0], e2 = cfma(l8, e1, ag[1]), e3 = cfma(l8, e2, ag[2]), la = cfma(l8, e3, ag[3]);
    f32x2 q[4];
#pragma unroll
    for (int k = 0; k < 4; ++k) q[k] = f32x2{__shfl(la.x, fr + 16 * k), __shfl(la.y, fr + 16 * k)};
    f32x2 E1 = q[0], E2 = cfma(l32, E1, q[1]), E3 = cfma(l32, E2, q[2]), AG = cfma(l32, E3, q[3]);
    const __amdgpu_buffer_rsrc_t er = __builtin_amdgcn_make_buffer_rsrc((void*)exch, 0, 16 * P * 16, 0x00020000);
    if (fq == 0 && c < 15)
      __builtin_amdgcn_raw_buffer_store_b128(u32x4{__float_as_uint(AG.x), tag.x, __float_as_uint(AG.y), tag.y}, er, (c * P + p) * 16, 0, 16);
    const f32x2 Ea = (fq & 1) ? E1 : f32x2{0.f, 0.f}, Eb = (fq & 1) ? E3 : E2;
    f32x2 E = (fq & 2) ? Eb : Ea;
    f32x2 car = {0.f, 0.f};
#pragma unroll
    for (int r = 0; r < 4; ++r) {
#pragma unroll
      for (int i = 0; i < 2; ++i) {
        CR[r][0][i] = *LDSP(bf16x8, cslot(r) + a0 + (32 * (w & 1) + 16 * i) * 128);
        CR[r][1][i] = *LDSP(bf16x8, cslot(r) + a1 + (32 * (w & 1) + 16 * i) * 128);
      }
      asm volatile("" : "+v"(CR[r][0][0]), "+v"(CR[r][0][1]), "+v"(CR[r][1][0]), "+v"(CR[r][1][1]));
    }
    if (c > 0) {
      u32x4 v[4];
      for (unsigned spin = 0;; ++spin) {
        bool ok = true;
#pragma unroll
        for (int i = 0; i < 4; ++i) {
          int j = min(fq + 4 * i, c - 1);
          v[i] = __builtin_amdgcn_raw_buffer_load_b128(er, (j * P + p) * 16, 0, 16);
          ok &= (v[i][1] == tag.x) & (v[i][3] == tag.y);
        }
        if (__all(ok) || spin > (1u << 20)) break;
        __builtin_amdgcn_s_sleep(1);
        asm volatile("" ::: "memory");
      }
#pragma unroll
      for (int i = 0; i < 4; ++i) {
        f32x2 vv = {__uint_as_float(v[i][0]), __uint_as_float(v[i][2])};
        if (fq + 4 * i < c) car = i == 0 ? vv : cfma(mu4, car, vv);
      }
      if (fq < c) car = cfma(mue, car, f32x2{0.f, 0.f});
      car.x += __shfl_xor(car.x, 16); car.y += __shfl_xor(car.y, 16);
      car.x += __shfl_xor(car.x, 32); car.y += __shfl_xor(car.y, 32);
    }
    f32x2 H = cfma(lq, car, E);
    f32x2 hs[4] = {H, cfma(l8, H, e1), cfma(l16, H, e2), cfma(l24, H, e3)};
#pragma unroll
    for (int i = 0; i < 8; ++i)
#pragma unroll
      for (int k = 0; k < 4; ++k) { hs[k] = cfma(lam, hs[k], u[8 * k + i]); u[8 * k + i] = hs[k]; }
#pragma unroll
    for (int i = 0; i < 4; ++i) {
      u32x4 vr, vi;
#pragma unroll
      for (int j = 0; j < 4; ++j) {
        vr[j] = pack2(u[8 * i + 2 * j].x, u[8 * i + 2 * j + 1].x);
        vi[j] = pack2(u[8 * i + 2 * j].y, u[8 * i + 2 * j + 1].y);
      }
      *LDSP(u32x4, (16 * w + fr) * IMG_PITCH + 64 * fq + 16 * i) = vr;
      *LDSP(u32x4, (64 + 16 * w + fr) * IMG_PITCH + 64 * fq + 16 * i) = vi;
    }
    WAIT_L0();
    BAR();
    {
      const __amdgpu_buffer_rsrc_t hr = __builtin_amdgcn_make_buffer_rsrc((void*)h, 0, 2048 * 2048 * 2, 0x00020000);
      const int a = (lane >> 2) & 3, bb = lane & 3;
      const int ib = (8 * fq + a) * IMG_PITCH + (32 * w + 4 * bb) * 2;
      bf16x8 o[8];
#pragma unroll
      for (int tb = 0; tb < 2; ++tb)
#pragma unroll
        for (int cq = 0; cq < 4; ++cq) {
          s16x4 lo = __builtin_amdgcn_ds_read_tr16_b64_v4i16(LDSP(s16x4, ib + (32 * cq) * IMG_PITCH + 32 * tb));
          s16x4 hi = __builtin_amdgcn_ds_read_tr16_b64_v4i16(LDSP(s16x4, ib + (32 * cq + 4) * IMG_PITCH + 32 * tb));
          o[4 * tb + cq] = __builtin_shufflevector(lo, hi, 0, 1, 2, 3, 4, 5, 6, 7);
        }
      SB0();
      const int ob = ((128 * c + 32 * w + fr) * 2048 + 128 * g + 8 * fq) * 2;
#pragma unroll
      for (int tb = 0; tb < 2; ++tb)
#pragma unroll
        for (int cq = 0; cq < 4; ++cq)
        {
          __builtin_amdgcn_raw_buffer_store_b128(__builtin_bit_cast(u32x4, o[4 * tb + cq]), hr, ob + tb * 65536 + cq * 64, 0, 16);
          *LDSP(bf16x8, 65536 + (cq >> 1) * 16384 + (32 * w + 16 * tb + fr) * 128 + (((4 * (cq & 1) + fq) ^ ((fr >> 1) & 7)) * 16)) = o[4 * tb + cq];
        }
    }
    WAIT_V(0);
    if (lane == 0) __hip_atomic_store(&flags[(c * 16 + g) * 16 + w], tag64, __ATOMIC_RELAXED, __HIP_MEMORY_SCOPE_AGENT);
    WAIT_L0();
    BAR();
  }
  f32x4 acc[2][4];
#pragma unroll
  for (int i = 0; i < 2; ++i)
#pragma unroll
    for (int j = 0; j < 4; ++j) acc[i][j] = f32x4{0, 0, 0, 0};
  bf16x8 F0[6], F1[6];
  auto rdh = [&](bf16x8* F, int hbase) {
#pragma unroll
    for (int j = 0; j < 4; ++j) F[2 + j] = *LDSP(bf16x8, hbase + (64 * wt + 16 * j) * 128);
  };
  auto rd = [&](bf16x8* F, int hbase, int cbase) {
#pragma unroll
    for (int i = 0; i < 2; ++i) F[i] = *LDSP(bf16x8, cbase + (32 * wd + 16 * i) * 128);
    rdh(F, hbase);
  };
  auto mm = [&](bf16x8* F) {
#pragma unroll
    for (int i = 0; i < 2; ++i)
#pragma unroll
      for (int j = 0; j < 4; ++j) acc[i][j] = __builtin_amdgcn_mfma_f32_16x16x32_bf16(F[i], F[2 + j], acc[i][j], 0, 0, 0);
  };
  int hl0 = a0 + 64 * wt * 128, hl1 = a1 + 64 * wt * 128, hh0 = hl0 + 65536, hh1 = hl1 + 65536;
  int cc0 = a0 + 32 * wd * 128 + 131072, cc1 = a1 + 32 * wd * 128 + 131072;
  asm volatile("" : "+v"(hl0), "+v"(hl1), "+v"(hh0), "+v"(hh1), "+v"(cc0), "+v"(cc1));
  auto rdh2 = [&](bf16x8* F, int base) {
#pragma unroll
    for (int j = 0; j < 4; ++j) F[2 + j] = *LDSP(bf16x8, base + 2048 * j);
  };
  auto rdo = [&](bf16x8* F, int hbase, int cbase) {
    F[0] = *LDSP(bf16x8, cbase); F[1] = *LDSP(bf16x8, cbase + 2048);
    rdh2(F, hbase);
  };
#pragma unroll
  for (int r = 4; r < 7; ++r)
#pragma unroll
    for (int i = 0; i < 2; ++i) {
      CR[r][0][i] = *LDSP(bf16x8, 40960 + (r - 4) * 8192 + a0 + (32 * wd + 16 * i) * 128);
      CR[r][1][i] = *LDSP(bf16x8, 40960 + (r - 4) * 8192 + a1 + (32 * wd + 16 * i) * 128);
    }
  rdo(F0, hh0, cc0);
  SB0();
  rdo(F1, hh1, cc1); mm(F0); ILV(6, 8)
  rdo(F0, hh0 + 16384, cc0 + 8192); mm(F1); ILV(6, 8)
  rdo(F1, hh1 + 16384, cc1 + 8192); mm(F0); ILV(6, 8)
  MFMA_DRAIN();
#pragma unroll
  for (int r = 4; r < 7; ++r) asm volatile("" : "+v"(CR[r][0][0]), "+v"(CR[r][0][1]), "+v"(CR[r][1][0]), "+v"(CR[r][1][1]));
  WAIT_L0();
  BAR();
  if (wave == 0) {
    for (unsigned spin = 0;; ++spin) {
      u64 v = __hip_atomic_load(&flags[(c * 16 + (lane >> 2)) * 16 + (lane & 3)], __ATOMIC_RELAXED, __HIP_MEMORY_SCOPE_AGENT);
      if (__all(v == tag64) || spin > (1u << 20)) break;
      __builtin_amdgcn_s_sleep(2);
    }
  }
  int hi_ = 0, ci_ = 2;
#pragma unroll 1
  for (int s = 2; s < 25; ++s) {
    int hb = hi_ * 16384, cb = ci_ < 4 ? 131072 + ci_ * 8192 : 98304 + (ci_ - 4) * 8192;
    BAR();
    SB0();
    rd(F0, hb + a0, cb + a0);
    mm(F1);
    ILV(6, 8)
    rd(F1, hb + a1, cb + a1);
    mm(F0);
    ILV(6, 8)
    WAIT_L0();
    hi_ = hi_ == 5 ? 0 : hi_ + 1; ci_ = (ci_ + 1) & 7;
  }
#pragma unroll
  for (int r = 0; r < 7; ++r) {
    const int sl = (25 + r + 4) % 6;
    BAR();
    SB0();
    rdh2(F0, sl < 4 ? hl0 + sl * 16384 : hh0 + (sl - 4) * 16384);
    mm(F1);
    ILV(4, 8)
    F0[0] = CR[r][0][0]; F0[1] = CR[r][0][1];
    rdh2(F1, sl < 4 ? hl1 + sl * 16384 : hh1 + (sl - 4) * 16384);
    mm(F0);
    ILV(4, 8)
    F1[0] = CR[r][1][0]; F1[1] = CR[r][1][1];
    WAIT_L0();
  }
  mm(F1);
  MFMA_DRAIN();
  const __amdgpu_buffer_rsrc_t yr = __builtin_amdgcn_make_buffer_rsrc((void*)y, 0, 2048 * 1024 * 4, 0x00020000);
#pragma unroll
  for (int dm = 0; dm < 2; ++dm) {
    int d = 64 * n + 32 * wd + 16 * dm + 4 * fq;
    float dk[4] = {__uint_as_float(ds[dm][0] << 16), __uint_as_float(ds[dm][0] & 0xffff0000u),
                   __uint_as_float(ds[dm][1] << 16), __uint_as_float(ds[dm][1] & 0xffff0000u)};
#pragma unroll
    for (int tn = 0; tn < 4; ++tn) {
      float xv[4] = {__uint_as_float(xs[dm][tn][0] << 16), __uint_as_float(xs[dm][tn][0] & 0xffff0000u),
                     __uint_as_float(xs[dm][tn][1] << 16), __uint_as_float(xs[dm][tn][1] & 0xffff0000u)};
      f32x4 o;
#pragma unroll
      for (int j = 0; j < 4; ++j) o[j] = 2.0f * acc[dm][tn][j] + bfround(xv[j] * dk[j]);
      __builtin_amdgcn_raw_buffer_store_b128(__builtin_bit_cast(u32x4, o), yr, ((128 * c + 64 * wt + 16 * tn + fr) * D + d) * 4, 0, 16);
    }
  }
  return;
  }
  {
    const int lw = wave - 4;
    int offC[2];
#pragma unroll
    for (int i = 0; i < 2; ++i) {
      int rho = 16 * lw + 8 * i + (lane >> 3);
      int chunk = (lane & 7) ^ ((rho >> 1) & 7);
      offC[i] = (64 * n + rho) * P + chunk * 8;
    }
    auto issueC = [&](int s, int slot_off) {
      const u16* Cb = ((s & 1) ? Cim : Cre) + coff(s);
#pragma unroll
      for (int i = 0; i < 2; ++i) GLDS(Cb + offC[i], slot_off + (2 * lw + i) * 1024);
    };
    {
      const u16* src[8];
#pragma unroll
      for (int i = 0; i < 4; ++i) {
        int rho = 32 * lw + 8 * i + (lane >> 3);
        int chunk = (lane & 7) ^ ((rho >> 1) & 7);
        int m = rho >> 4, i16 = rho & 15;
        int tau = 32 * (i16 >> 2) + 4 * m + (i16 & 3);
        src[i] = x + (size_t)(128 * c + tau) * D + chunk * 8;
        int p = 64 * g + 16 * (rho >> 5) + (rho & 15);
        src[4 + i] = ((rho & 16) ? Bim : Bre) + (size_t)p * D + chunk * 8;
      }
      auto issue = [&](int s0) {
        int buf = (s0 & 3) * ST1;
        int s = (s0 + koff) & 15;
#pragma unroll
        for (int i = 0; i < 4; ++i) GLDS(src[i] + s * 64, buf + (4 * lw + i) * 1024);
#pragma unroll
        for (int i = 0; i < 4; ++i) GLDS(src[4 + i] + s * 64, buf + 16384 + (4 * lw + i) * 1024);
      };
      issue(0); issue(1); issue(2);
      for (int s = 0; s < 6; ++s) { WAIT_V(16); BAR(); issue(s + 3); }
      WAIT_V(16); BAR(); issue(9);
      {
        const char* p1 = (const char*)Cre + b * 8192 + lw * 2048 + lane * 16;
        const char* p2 = (const char*)Cim + b * 8192 + lw * 2048 + lane * 16;
        GLDS(p1, 131072 + lw * 1024); GLDS(p1 + 1024, 131072 + lw * 1024);
        GLDS(p2, 131072 + lw * 1024); GLDS(p2 + 1024, 131072 + lw * 1024);
      }
      WAIT_V(20); BAR(); issue(10);
      WAIT_V(20); BAR(); issue(11);
      WAIT_V(20); BAR(); issue(12);
      WAIT_V(16); BAR(); issue(13);
      WAIT_V(16); BAR(); issue(14);
      WAIT_V(16); BAR(); issue(15);
      issueC(25, cslot(0)); issueC(26, cslot(1)); issueC(27, cslot(2)); issueC(28, cslot(3));
      WAIT_V(24); BAR();
      WAIT_V(16); BAR();
      WAIT_V(0); BAR();
    }
    const u16* srcH[4];
#pragma unroll
    for (int i = 0; i < 4; ++i) {
      int rho = 32 * lw + 8 * i + (lane >> 3);
      int chunk = (lane & 7) ^ ((rho >> 1) & 7);
      srcH[i] = h + (size_t)(128 * c + rho) * 2048 + chunk * 8;
    }
    auto issueH = [&](int s, int bi) {
      const int ho = coff(s) * 2 + (s & 1) * 64;
#pragma unroll
      for (int i = 0; i < 4; ++i) GLDS_SC1(srcH[i] + ho, bi * 16384 + (4 * lw + i) * 1024);
    };
    issueC(29, 40960); issueC(30, 49152); issueC(31, 57344);
    BAR();
    issueC(4, cslot(4)); issueC(5, cslot(5)); issueC(6, cslot(6)); issueC(7, cslot(7));
    BAR();
    issueC(0, cslot(0)); issueC(1, cslot(1)); issueC(2, cslot(2)); issueC(3, cslot(3));
    WAIT_V(0);
    BAR();
    if (lw == 0) {
      const bool first = (lane >> 2) * 64 == coff(2) || (lane >> 2) * 64 == coff(4);
      for (unsigned spin = 0;; ++spin) {
        u64 v = __hip_atomic_load(&flags[(c * 16 + (lane >> 2)) * 16 + (lane & 3)], __ATOMIC_RELAXED, __HIP_MEMORY_SCOPE_AGENT);
        if (__all(v == tag64 || !first) || spin > (1u << 20)) break;
        __builtin_amdgcn_s_sleep(2);
      }
    }
    BAR();
    issueH(2, 0);
    issueH(3, 1);
    issueH(4, 2);
    issueH(5, 3);
    WAIT_V(12); BAR();
    issueH(6, 4);
    issueC(8, cslot(0));
    issueH(7, 5);
    issueC(9, cslot(1));
    WAIT_V(20); BAR();
    issueH(8, 0);
    issueC(10, cslot(2));
    WAIT_V(22); BAR();
    issueH(9, 1);
    issueC(11, cslot(3));
    WAIT_V(24); BAR();
    issueH(10, 2);
    issueC(12, cslot(4));
    WAIT_V(26); BAR();
    issueH(11, 3);
    issueC(13, cslot(5));
    WAIT_V(26); BAR();
    issueH(12, 4);
    issueC(14, cslot(6));
    WAIT_V(26); BAR();
    issueH(13, 5);
    issueC(15, cslot(7));
    WAIT_V(26); BAR();
    issueH(14, 0);
    issueC(16, cslot(0));
    WAIT_V(26); BAR();
    issueH(15, 1);
    issueC(17, cslot(1));
    WAIT_V(26); BAR();
    issueH(16, 2);
    issueC(18, cslot(2));
    WAIT_V(26); BAR();
    issueH(17, 3);
    issueC(19, cslot(3));
    WAIT_V(26); BAR();
    issueH(18, 4);
    issueC(20, cslot(4));
    WAIT_V(26); BAR();
    issueH(19, 5);
    issueC(21, cslot(5));
    WAIT_V(26); BAR();
    issueH(20, 0);
    issueC(22, cslot(6));
    WAIT_V(26); BAR();
    issueH(21, 1);
    issueC(23, cslot(7));
    WAIT_V(26); BAR();
    issueH(22, 2);
    issueC(24, cslot(0));
    WAIT_V(26); BAR();
    issueH(23, 3);
    WAIT_V(24); BAR();
    issueH(24, 4);
    WAIT_V(22); BAR();
    issueH(25, 5);
    WAIT_V(20); BAR();
    issueH(26, 0);
    WAIT_V(18); BAR();
    issueH(27, 1);
    WAIT_V(16); BAR();
    issueH(28, 2);
    WAIT_V(16); BAR();
    issueH(29, 3);
    WAIT_V(16); BAR();
    issueH(30, 4);
    WAIT_V(16); BAR();
    issueH(31, 5);
    WAIT_V(16); BAR();
    WAIT_V(12); BAR();
    WAIT_V(8); BAR();
    WAIT_V(4); BAR();
    WAIT_V(0); BAR();
    return;
  }
}

extern "C" void kernel_launch(void* const* d_in, const int* in_sizes, int n_in, void* d_out, int out_size,
                              void* d_ws, size_t ws_size, hipStream_t stream) {
  const u16* x = (const u16*)d_in[0];
  const u16* Lre = (const u16*)d_in[1];
  const u16* Lim = (const u16*)d_in[2];
  const u16* Bre = (const u16*)d_in[3];
  const u16* Bim = (const u16*)d_in[4];
  const u16* Cre = (const u16*)d_in[5];
  const u16* Cim = (const u16*)d_in[6];
  const u16* logdt = (const u16*)d_in[7];
  const u16* Dskip = (const u16*)d_in[8];
  float* out = (float*)d_out;
  char* ws = (char*)d_ws;
  u32x4* exch = (u32x4*)ws;
  u64* flags = (u64*)(ws + 262144);
  u16* h = (u16*)(ws + (1 << 20));
  (void)hipFuncSetAttribute((const void*)s5_fused, hipFuncAttributeMaxDynamicSharedMemorySize, LDS_BYTES);
  s5_fused<<<256, 512, LDS_BYTES, stream>>>(x, Bre, Bim, Cre, Cim, Lre, Lim, logdt, Dskip, h, exch, flags, out);
}
```
